```python
import torch

def _softmax(x, axis=-1):
    return torch.softmax(x, dim=axis)
def _gelu(x):
    return torch.nn.functional.gelu(x, approximate="tanh")
def _silu(x):
    return torch.nn.functional.silu(x)
def _sigmoid(x):
    return torch.sigmoid(x)
def _layernorm(x, axis=-1, eps=1e-5):
    m = x.mean(dim=axis, keepdim=True)
    v = x.var(dim=axis, keepdim=True, unbiased=False)
    return (x - m) / torch.sqrt(v + eps)
def _scatter_add(tgt, idx, src, axis=0):
    return tgt.clone().index_add_(axis, idx, src)
def _segment_sum(src, seg, n):
    out = torch.zeros((n,) + src.shape[1:], dtype=src.dtype)
    return out.index_add_(0, seg, src)
def _pad(x, pw):
    flat = tuple(v for pair in reversed(pw) for v in pair)
    return torch.nn.functional.pad(x, flat)

kernel_name = 'parallel_sum__sliding_block_cached__dense_activation_relu__decode'

def setup_inputs():

    _w = (lambda a: (lambda t: t.to(torch.bfloat16) if t.is_floating_point() else t)(torch.as_tensor(a)))
    def _A_setup():
        B, Sq, D = 8, 1, 512
        Hq, Hkv, Sk, Dh = 32, 8, 8192, 128
        assert Hq % Hkv == 0, (Hq, Hkv)
        return {
            "x": _w((lambda *s: torch.randn(*s, dtype=torch.bfloat16) * 0.05)(B, Sq, D)),
            "k_cache": _w((lambda *s: torch.randn(*s, dtype=torch.bfloat16) * 3.2)(B, Hkv, Sk, Dh)),
            "v_cache": _w((lambda *s: torch.randn(*s, dtype=torch.bfloat16) * 1.6)(B, Hkv, Sk, Dh)),
            "wq": _w((lambda *s: torch.randn(*s, dtype=torch.bfloat16) * 3.2)(D, Hq * Dh) * (D ** -0.5)),
            "wo": _w((lambda *s: torch.randn(*s, dtype=torch.bfloat16) * 1.6)(Hq * Dh, D) * ((Hq * Dh) ** -0.5)),
        }
    _A_in = _A_setup()

    def _B_setup():
        B, S, D, Dff = 8, 1, 512, 1792
        return {
            "x": _w((lambda *s: torch.randn(*s, dtype=torch.bfloat16) * 0.05)(B, S, D)),
            "w1": _w((lambda *s: torch.randn(*s, dtype=torch.bfloat16) * 0.05)(D, Dff)),
            "w2": _w((lambda *s: torch.randn(*s, dtype=torch.bfloat16) * 0.05)(Dff, D)),
        }
    _B_in = _B_setup()

    return {
        "A_x": _A_in["x"],
        "A_k_cache": _A_in["k_cache"],
        "A_v_cache": _A_in["v_cache"],
        "A_wq": _A_in["wq"],
        "A_wo": _A_in["wo"],
        "B_x": _B_in["x"],
        "B_w1": _B_in["w1"],
        "B_w2": _B_in["w2"],
    }

def reference(A_x, A_k_cache, A_v_cache, A_wq, A_wo, B_x, B_w1, B_w2):
    def _A_ref(x, k_cache, v_cache, wq, wo):
        B, Sq, D = x.shape
        Hq, Dh = 32, 128
        Hkv, Sk = k_cache.shape[1], k_cache.shape[2]
        W = min(4096, max(256, Sk // 4))
        rep = Hq // Hkv
        q = (lambda x, *d: x.permute(*d))((x @ wq).reshape(B, Sq, Hq, Dh), 0, 2, 1, 3)
        k = (lambda x, n, axis: torch.repeat_interleave(x, n, dim=axis))(k_cache[:, :, Sk - W:, :], rep, axis=1)
        v = (lambda x, n, axis: torch.repeat_interleave(x, n, dim=axis))(v_cache[:, :, Sk - W:, :], rep, axis=1)
        scores = torch.einsum("bhqd,bhkd->bhqk", q, k) * (Dh ** -0.5)
        p = _softmax(scores, axis=-1)
        o = torch.einsum("bhqk,bhkd->bhqd", p, v)
        return (lambda x, *d: x.permute(*d))(o, 0, 2, 1, 3).reshape(B, Sq, Hq * Dh) @ wo

    def _B_ref(x, w1, w2):
        h = torch.einsum("bsd,df->bsf", x, w1)
        zero = torch.zeros_like(h)
        h = torch.maximum(h, zero)
        return torch.einsum("bsf,fd->bsd", h, w2)

    x = A_x
    xn = _layernorm(x, axis=-1)
    return x + _A_ref(xn, A_k_cache, A_v_cache, A_wq, A_wo) + _B_ref(xn, B_w1, B_w2)
```

```cpp
#include <hip/hip_runtime.h>
#include <hip/hip_bf16.h>
#include <stdint.h>
#define MIMIC 1
typedef unsigned short u16;
typedef unsigned v4u __attribute__((ext_vector_type(4)));
__device__ __forceinline__ float bf2f(u16 b) { return __uint_as_float((unsigned)b << 16); }
__device__ __forceinline__ u16 f2bf(float f) { unsigned u = __float_as_uint(f); u += 0x7FFFu + ((u >> 16) & 1u); return (u16)(u >> 16); }
__device__ __forceinline__ float rb(float f) { return MIMIC ? bf2f(f2bf(f)) : f; }
constexpr int B = 8, D = 512, HQ = 32, HKV = 8, SK = 8192, DH = 128, W = 2048, DFF = 1792, NS = 8;

__device__ void layernorm_all(const u16* x, float* xn  ) {
  const int tid = threadIdx.x, lane = tid & 63, wave = tid >> 6, nw = blockDim.x >> 6;
  for (int b = wave; b < B; b += nw) {
    float v[8]; float s = 0.f;
    for (int i = 0; i < 8; ++i) { v[i] = bf2f(x[b * D + lane * 8 + i]); s += v[i]; }
    for (int o = 32; o; o >>= 1) s += __shfl_xor(s, o);
    const float mf = s / D;
    float q = 0.f;
    for (int i = 0; i < 8; ++i) { float d = v[i] - mf; q += d * d; }
    for (int o = 32; o; o >>= 1) q += __shfl_xor(q, o);
    const float m = rb(mf), var = rb(q / D);
    const float den = rb(sqrtf(rb(var + 1e-5f)));
    for (int i = 0; i < 8; ++i) xn[b * D + lane * 8 + i] = rb(rb(v[i] - m) / den);
  }
}
__global__ void __launch_bounds__(256) k1(const u16* x, const u16* wq, const u16* w1, float* q, float* h) {
  __shared__ float xn[B * D];
  __shared__ float red[4][8][64];
  layernorm_all(x, xn);
  __syncthreads();
  const int lane = threadIdx.x & 63, wave = threadIdx.x >> 6;
  const bool isq = blockIdx.x < 64;
  const u16* Wm = isq ? wq : w1; const int ld = isq ? 4096 : DFF; const int col = (isq ? blockIdx.x : blockIdx.x - 64) * 64 + lane;
  float acc[8] = {0};
  for (int k = wave * 128; k < wave * 128 + 128; ++k) { float w = bf2f(Wm[(size_t)k * ld + col]); for (int b = 0; b < 8; ++b) acc[b] += xn[b * D + k] * w; }
  for (int b = 0; b < 8; ++b) red[wave][b][lane] = acc[b];
  __syncthreads();
  for (int b = wave; b < 8; b += 4) { float s = red[0][b][lane] + red[1][b][lane] + red[2][b][lane] + red[3][b][lane]; s = rb(s);
    if (isq) q[b * 4096 + col] = s; else h[b * DFF + col] = fmaxf(s, 0.f); }
}
__global__ void __launch_bounds__(256) k2(const u16* kc, const u16* vc, const float* q, float* sc_out, float* po, float* pml) {
  constexpr int KB = W / NS;
  __shared__ float qs[4 * DH];
  __shared__ float s[4][KB];
  __shared__ float red[4][4];
  __shared__ float ored[16][4][DH];
  const int sp = blockIdx.x, kvh = blockIdx.y, b = blockIdx.z, tid = threadIdx.x, lane = tid & 63, wave = tid >> 6;
  for (int i = tid; i < 512; i += 256) qs[i] = q[b * 4096 + kvh * 512 + i];
  __syncthreads();
  const size_t base = ((size_t)(b * HKV + kvh) * SK + (SK - W) + sp * KB) * DH;
  { const int r = tid >> 4, c = tid & 15;
    for (int k0 = 0; k0 < KB; k0 += 16) { const int key = k0 + r;
      v4u kv = *(const v4u*)(kc + base + (size_t)key * DH + c * 8);
      float a[4] = {0, 0, 0, 0};
      for (int j = 0; j < 4; ++j) { float lo = __uint_as_float(kv[j] << 16), hi = __uint_as_float(kv[j] & 0xffff0000u);
        for (int g = 0; g < 4; ++g) a[g] += lo * qs[g * DH + c * 8 + 2 * j] + hi * qs[g * DH + c * 8 + 2 * j + 1]; }
      for (int g = 0; g < 4; ++g) { for (int o = 8; o; o >>= 1) a[g] += __shfl_xor(a[g], o); }
      if (c == 0) for (int g = 0; g < 4; ++g) s[g][key] = rb(rb(a[g]) * 0.088388347648318440f); } }
  __syncthreads();
  { const int g = wave; float m = -INFINITY;
    for (int i = lane; i < KB; i += 64) m = fmaxf(m, s[g][i]);
    for (int o = 32; o; o >>= 1) m = fmaxf(m, __shfl_xor(m, o));
    float l = 0.f;
    for (int i = lane; i < KB; i += 64) { float p = __expf(s[g][i] - m); s[g][i] = p; l += p; }
    for (int o = 32; o; o >>= 1) l += __shfl_xor(l, o);
    if (lane == 0) { const size_t slot = ((size_t)(b * HQ + kvh * 4 + g) * NS + sp); pml[2 * slot] = m; pml[2 * slot + 1] = l; } }
  __syncthreads();
  { const int r = tid >> 4, c = tid & 15; float acc[4][8] = {};
    for (int k0 = 0; k0 < KB; k0 += 16) { const int key = k0 + r;
      v4u vv = *(const v4u*)(vc + base + (size_t)key * DH + c * 8);
      for (int j = 0; j < 4; ++j) { float lo = __uint_as_float(vv[j] << 16), hi = __uint_as_float(vv[j] & 0xffff0000u);
        for (int g = 0; g < 4; ++g) { float p = s[g][key]; acc[g][2 * j] += p * lo; acc[g][2 * j + 1] += p * hi; } } }
    for (int g = 0; g < 4; ++g) for (int j = 0; j < 8; ++j) ored[r][g][c * 8 + j] = acc[g][j]; }
  __syncthreads();
  for (int i = tid; i < 512; i += 256) { float t = 0.f; for (int r = 0; r < 16; ++r) t += ored[r][i >> 7][i & 127];
    po[((size_t)(b * HQ + kvh * 4 + (i >> 7)) * NS + sp) * DH + (i & 127)] = t; }
}
__global__ void __launch_bounds__(1024) k3(const u16* x, const u16* wo, const u16* w2, const float* po, const float* pml, const float* h, u16* out) {
  __shared__ float o[4096];
  __shared__ float red[16][2][64];
  const int b = blockIdx.y, tid = threadIdx.x, lane = tid & 63, wave = tid >> 6, col = blockIdx.x * 64 + lane;
  for (int i = tid; i < 4096; i += 1024) { const int hq = i >> 7, d = i & 127; const float* ml = pml + (size_t)(b * HQ + hq) * NS * 2;
    float M = -INFINITY; for (int s = 0; s < NS; ++s) M = fmaxf(M, ml[2 * s]);
    float L = 0.f, t = 0.f; for (int s = 0; s < NS; ++s) { float w = __expf(ml[2 * s] - M); L += w * ml[2 * s + 1]; t += w * po[((size_t)(b * HQ + hq) * NS + s) * DH + d]; }
    o[i] = rb(t / L); }
  __syncthreads();
  float a = 0.f, f = 0.f;
  for (int k = wave * 256; k < wave * 256 + 256; ++k) a += o[k] * bf2f(wo[(size_t)k * D + col]);
  for (int k = wave * 112; k < wave * 112 + 112; ++k) f += h[b * DFF + k] * bf2f(w2[(size_t)k * D + col]);
  red[wave][0][lane] = a; red[wave][1][lane] = f;
  __syncthreads();
  if (wave == 0) { float A = 0.f, F = 0.f; for (int w = 0; w < 16; ++w) { A += red[w][0][lane]; F += red[w][1][lane]; }
    out[b * D + col] = f2bf(rb(bf2f(x[b * D + col]) + rb(A)) + rb(F)); }
}
extern "C" void kernel_launch(void* const* d_in, const int* in_sizes, int n_in, void* d_out, int out_size, void* d_ws, size_t ws_size, hipStream_t stream) {
  const u16* x = (const u16*)d_in[0]; const u16* kc = (const u16*)d_in[1]; const u16* vc = (const u16*)d_in[2];
  const u16* wq = (const u16*)d_in[3]; const u16* wo = (const u16*)d_in[4]; const u16* w1 = (const u16*)d_in[6]; const u16* w2 = (const u16*)d_in[7];
  float* ws = (float*)d_ws; float* q = ws; float* h = q + B * 4096; float* pml = h + B * DFF; float* po = pml + B * HQ * NS * 2;
  k1<<<64 + 28, 256, 0, stream>>>(x, wq, w1, q, h);
  k2<<<dim3(NS, HKV, B), 256, 0, stream>>>(kc, vc, q, nullptr, po, pml);
  k3<<<dim3(8, B), 1024, 0, stream>>>(x, wo, w2, po, pml, h, (u16*)d_out);
}
```

```cpp
#include <hip/hip_runtime.h>
#include <hip/hip_bf16.h>
#include <stdint.h>

#define CHL 4
#define CHH 4
#define QTRIG 8
#define PVPOS 12
#define KPRE 2
#define WPRE 8
#define W0 4
#define N0 0
#define VPRE 4
#define WO_LATE 2
#define WO_CNT 4
#define OSLEEP 8
#define YSLEEP 8
#define OPRE 8
#define YPRE 16
#define AUX_KV 2
#define AUX_W 2
#define AUX_ST 16
#define AUX_LD 16
#define WS_BASE 0

typedef unsigned v4u __attribute__((ext_vector_type(4)));
typedef unsigned v2u __attribute__((ext_vector_type(2)));
typedef float f32x4 __attribute__((ext_vector_type(4)));
typedef float f32x2 __attribute__((ext_vector_type(2)));
typedef short v4s __attribute__((ext_vector_type(4)));
typedef __bf16 bf16x8 __attribute__((ext_vector_type(8)));
typedef __bf16 bf16x2 __attribute__((ext_vector_type(2)));
#define LAS __attribute__((address_space(3)))
typedef __amdgpu_buffer_rsrc_t rsrc_t;

constexpr int SK = 8192, WIN = 2048;
constexpr unsigned SPIN_CAP = 1u << 16;
constexpr float LOG2E = 1.4426950408889634f;
constexpr int WS_EPOCH = 0;
constexpr int WS_QG = 4096;
constexpr int WS_HG = WS_QG + 256 * 512;
constexpr int WS_ML = WS_HG + 128 * 512;
constexpr int WS_OG = WS_ML + 64 * 512;
constexpr int WS_YG = WS_OG + 256 * 2048;
constexpr int WS_END = WS_YG + 256 * 2048;
#define STAMP(k) do {} while (0)
#define STAMP_OUT() do {} while (0)
constexpr int XN_LD = 1040;
constexpr int L_XN = 0;
constexpr int W_LD = 144;
constexpr int S_LD = 80;
constexpr int L_WQ = 8448;
constexpr int L_RD = L_WQ + 512 * S_LD;
constexpr int L_WO = L_RD + 8192;
constexpr int A_LD = 400;
constexpr int L_A = L_WO + 192 * W_LD;
constexpr int L_Q = L_A + 8 * A_LD;
constexpr int L_ML = L_Q + 1024;
constexpr int L_SC = L_ML + 256;
constexpr int L_CNT = L_SC + 128;
constexpr int L_OR = L_CNT + 64;
constexpr int L_END = L_OR + 16384;
constexpr int LDS_BYTES = 112 * 1024;
static_assert(L_END <= LDS_BYTES && L_A % 16 == 0 && L_Q % 16 == 0 && L_OR % 16 == 0, "lds");
enum { C_XN = 0, C_Q = 1, C_ML = 2, C_V = 3, C_K = 4, C_RD = 5 };

__device__ __forceinline__ rsrc_t mkrs(const void* p, int bytes) { return __builtin_amdgcn_make_buffer_rsrc((void*)p, 0, bytes, 0x00020000); }
template <int N> __device__ __forceinline__ void waitvm() { asm volatile("s_waitcnt vmcnt(%0)" ::"n"(N) : "memory"); }
__device__ __forceinline__ void wg_barrier() { asm volatile("s_waitcnt lgkmcnt(0)\n\ts_barrier" ::: "memory"); }
__device__ __forceinline__ void pin() { __builtin_amdgcn_sched_barrier(0); }
__device__ __forceinline__ void reload() { asm volatile("" ::: "memory"); }
__device__ __forceinline__ float asf(unsigned u) { return __builtin_bit_cast(float, u); }
__device__ __forceinline__ unsigned asu(float f) { return __builtin_bit_cast(unsigned, f); }
__device__ __forceinline__ float lo16(unsigned u) { return asf(u << 16); }
__device__ __forceinline__ float hi16(unsigned u) { return asf(u & 0xffff0000u); }
__device__ __forceinline__ float rbf(float f) { return (float)(__bf16)f; }
__device__ __forceinline__ unsigned pk(float a, float b) { bf16x2 v = {(__bf16)a, (__bf16)b}; return __builtin_bit_cast(unsigned, v); }
template <int CTRL> __device__ __forceinline__ float dpp(float x) { return asf(__builtin_amdgcn_mov_dpp(asu(x), CTRL, 0xf, 0xf, true)); }
constexpr int XOR1 = 0xB1, XOR2 = 0x4E, HMIR = 0x141, ROR4 = 0x124, ROR8 = 0x128;
__device__ __forceinline__ float sum16(float x) { auto s = __builtin_amdgcn_permlane16_swap(asu(x), asu(x), false, false); return asf(s[0]) + asf(s[1]); }
__device__ __forceinline__ float sum32(float x) { auto s = __builtin_amdgcn_permlane32_swap(asu(x), asu(x), false, false); return asf(s[0]) + asf(s[1]); }
__device__ __forceinline__ float tsum32(float a, float b) { auto s = __builtin_amdgcn_permlane32_swap(asu(a), asu(b), false, false); return asf(s[0]) + asf(s[1]); }
__device__ __forceinline__ float tsum16(float a, float b) { auto s = __builtin_amdgcn_permlane16_swap(asu(a), asu(b), false, false); return asf(s[0]) + asf(s[1]); }
__device__ __forceinline__ float max16(float x) { auto s = __builtin_amdgcn_permlane16_swap(asu(x), asu(x), false, false); return fmaxf(asf(s[0]), asf(s[1])); }
__device__ __forceinline__ float max32(float x) { auto s = __builtin_amdgcn_permlane32_swap(asu(x), asu(x), false, false); return fmaxf(asf(s[0]), asf(s[1])); }
__device__ __forceinline__ float rowsum(float x) { x += dpp<XOR1>(x); x += dpp<XOR2>(x); x += dpp<ROR4>(x); x += dpp<ROR8>(x); return x; }
__device__ __forceinline__ float wavesum(float x) { return sum32(sum16(rowsum(x))); }
__device__ __forceinline__ float ex2(float x) { return __builtin_amdgcn_exp2f(x); }
__device__ __forceinline__ f32x4 mfma(v4u a, v4u b, f32x4 c) { return __builtin_amdgcn_mfma_f32_16x16x32_bf16(__builtin_bit_cast(bf16x8, a), __builtin_bit_cast(bf16x8, b), c, 0, 0, 0); }
__device__ __forceinline__ unsigned plo(unsigned a, unsigned b) { return __builtin_amdgcn_perm(b, a, 0x05040100u); }
__device__ __forceinline__ unsigned phi(unsigned a, unsigned b) { return __builtin_amdgcn_perm(b, a, 0x07060302u); }
__device__ __forceinline__ bool tagok(v4u v, unsigned tag) { return (v.y == tag) & (v.w == tag); }
__device__ __forceinline__ v4u ldw(rsrc_t r, int vo, int so) { return __builtin_amdgcn_raw_buffer_load_b128(r, vo, so, AUX_W); }
__device__ __forceinline__ v4u ldg(rsrc_t r, int vo, int so) { return __builtin_amdgcn_raw_buffer_load_b128(r, vo, so, AUX_LD); }
__device__ __forceinline__ void stg(rsrc_t r, int vo, int so, unsigned a, unsigned b, unsigned tag) { __builtin_amdgcn_raw_buffer_store_b128(v4u{a, tag, b, tag}, r, vo, so, AUX_ST); }
__device__ __forceinline__ void signal(LAS unsigned char* L, int k, int lane) {
  asm volatile("s_waitcnt lgkmcnt(0)" ::: "memory");
  if (lane == 0) __hip_atomic_fetch_add((LAS unsigned*)(L + L_CNT + 4 * k), 1u, __ATOMIC_RELAXED, __HIP_MEMORY_SCOPE_WORKGROUP);
}
template <int SLP> __device__ __forceinline__ void await(LAS unsigned char* L, int k, unsigned n) {
#pragma unroll 1
  for (unsigned spin = 0; spin < (SPIN_CAP << 4); ++spin) {
    if ((unsigned)__builtin_amdgcn_readfirstlane(__hip_atomic_load((LAS unsigned*)(L + L_CNT + 4 * k), __ATOMIC_RELAXED, __HIP_MEMORY_SCOPE_WORKGROUP)) >= n) break;
    __builtin_amdgcn_s_sleep(SLP);
  }
  reload();
}
template <int NS, int WLD, int ALD> __device__ __forceinline__ f32x4 tile_mm(LAS unsigned char* wt, LAS unsigned char* act, int lane) {
  f32x4 d = {0.f, 0.f, 0.f, 0.f};
  LAS unsigned char* wp = wt + ((lane >> 4) * 8 + ((lane >> 2) & 3)) * WLD + (lane & 3) * 8;
  LAS unsigned char* ap = act + (lane & 7) * ALD + (lane >> 4) * 16;
#pragma unroll
  for (int s = 0; s < NS; ++s) {
    const v2u lo = __builtin_bit_cast(v2u, __builtin_amdgcn_ds_read_tr16_b64_v4i16((LAS v4s*)(wp + s * 32 * WLD)));
    const v2u hi = __builtin_bit_cast(v2u, __builtin_amdgcn_ds_read_tr16_b64_v4i16((LAS v4s*)(wp + (s * 32 + 4) * WLD)));
    d = mfma(v4u{lo.x, lo.y, hi.x, hi.y}, *(LAS v4u*)(ap + s * 64), d);
  }
  return d;
}
template <int SLP> __device__ __forceinline__ void gather2(v4u& a0, v4u& a1, rsrc_t rws, int vo, int so0, int so1, unsigned tag) {
#pragma unroll 1
  for (unsigned spin = 0; spin < SPIN_CAP; ++spin) {
    reload();
    a0 = ldg(rws, vo, so0); a1 = ldg(rws, vo, so1);
    if (__all(tagok(a0, tag) & tagok(a1, tag))) break;
    __builtin_amdgcn_s_sleep(SLP);
  }
}

__global__ void __launch_bounds__(1024, 4)
fused_decode(const unsigned char* __restrict__ x, const unsigned char* __restrict__ kc, const unsigned char* __restrict__ vc,
             const unsigned char* __restrict__ wq, const unsigned char* __restrict__ wo, const unsigned char* __restrict__ w1,
             const unsigned char* __restrict__ w2, unsigned char* out, unsigned char* ws) {
  __shared__ __attribute__((aligned(16))) unsigned char smem[LDS_BYTES];
  LAS unsigned char* L = (LAS unsigned char*)smem;
  const int bid = blockIdx.x, kvh = bid & 7, r = bid >> 3, b = r & 7, j = r >> 3;
  const int tid = threadIdx.x, lane = tid & 63, w = __builtin_amdgcn_readfirstlane(tid >> 6);
  const int g = lane >> 4, vv = lane * 16;
  const bool do_q = r < 16, heavy = r < 23;
  STAMP(0);
  asm volatile("" ::"s"(x), "s"(kc), "s"(vc), "s"(wq), "s"(wo), "s"(w1), "s"(w2), "s"(out), "s"(ws));
  STAMP(1);

  if (w >= 8) {
    const int dw = w - 8;
    const size_t kvoff = ((size_t)(b * 8 + kvh) * SK + (SK - WIN) + j * 512) * 256;
    const rsrc_t rk = mkrs(kc + kvoff, 131072), rv = mkrs(vc + kvoff, 131072);
    const int rot = (int)(((unsigned)bid * 2654435761u) >> 20) + dw;
    const int vk = ((lane >> 2) & 3) * 256 + g * 64 + (lane & 3) * 16;
    v4u K[16], V[16];
#define PIECE(i) ((((i) * 8 + rot) & 127) << 10)
#define LDK(i) K[i] = __builtin_amdgcn_raw_buffer_load_b128(rk, vk, PIECE(i), AUX_KV)
#define LDV(i) V[i] = __builtin_amdgcn_raw_buffer_load_b128(rv, vv, PIECE(i), AUX_KV)
#define WAITV() do { if (heavy) waitvm<CHH - 1>(); else waitvm<CHL - 1>(); } while (0)
#pragma unroll
    for (int i = 0; i < KPRE; ++i) { LDK(i); pin(); }
    __builtin_amdgcn_s_barrier();
    STAMP(9);
#pragma unroll
    for (int i = KPRE; i < 16; ++i) { if (i >= W0 && i < N0) { if (!heavy) waitvm<W0 - 1>(); } if (i >= CHL) WAITV(); if (i == CHH) STAMP(10); if (i == 8) STAMP(11); if (i == 12) STAMP(12); LDK(i); if (i == QTRIG) signal(L, C_K, lane); pin(); }
    STAMP(2);
#pragma unroll
    for (int i = 0; i < VPRE; ++i) { WAITV(); LDV(i); pin(); }
    STAMP(3);
    await<1>(L, C_Q, 1);
    STAMP(4);
    const v4u qb = *(LAS v4u*)(L + L_Q + vk);
    float s[16];
#pragma unroll
    for (int i4 = 0; i4 < 4; ++i4) {
#pragma unroll
      for (int i = 4 * i4; i < 4 * i4 + 4; i += 2) {
        const f32x4 d = mfma(K[i], qb, f32x4{0.f, 0.f, 0.f, 0.f}), e = mfma(K[i + 1], qb, f32x4{0.f, 0.f, 0.f, 0.f});
        const float v0 = (dpp<0x00>(d[0]) + dpp<0x55>(d[1])) + (dpp<0xAA>(d[2]) + dpp<0xFF>(d[3]));
        const float v1 = (dpp<0x00>(e[0]) + dpp<0x55>(e[1])) + (dpp<0xAA>(e[2]) + dpp<0xFF>(e[3]));
        const unsigned u = pk(v0, v1);
        const unsigned t = pk(lo16(u) * 0.088388347648318440f, hi16(u) * 0.088388347648318440f);
        s[i] = lo16(t); s[i + 1] = hi16(t);
      }
      pin();
      if (i4 & 1) { LDV(VPRE + (i4 >> 1)); pin(); }
    }
    float mw = fmaxf(s[0], s[1]);
#pragma unroll
    for (int i = 2; i < 16; i += 2) mw = fmaxf(fmaxf(mw, s[i]), s[i + 1]);
    mw = max32(max16(mw));
    float lw = 0.f;
    {
      const float mc = -mw * LOG2E;
#pragma unroll
      for (int i = 0; i < 16; ++i) { s[i] = ex2(fmaf(s[i], LOG2E, mc)); lw += s[i]; }
    }
    lw = sum32(sum16(lw));
    const v4u pa0 = {pk(s[0], s[1]), pk(s[2], s[3]), pk(s[4], s[5]), pk(s[6], s[7])};
    const v4u pa1 = {pk(s[8], s[9]), pk(s[10], s[11]), pk(s[12], s[13]), pk(s[14], s[15])};
    if ((lane & 0x33) == 0) *(LAS f32x2*)(L + L_ML + lane * 16 + dw * 8) = f32x2{mw, lw};
    signal(L, C_ML, lane);
    STAMP(5);
    pin();
    f32x4 acc[8];
#pragma unroll
    for (int e = 0; e < 8; ++e) acc[e] = f32x4{0.f, 0.f, 0.f, 0.f};
#define PVGROUP(G, PA) do { _Pragma("unroll") for (int d = 0; d < 4; ++d) { \
    const v4u be_ = {plo(V[8*G+0][d], V[8*G+1][d]), plo(V[8*G+2][d], V[8*G+3][d]), plo(V[8*G+4][d], V[8*G+5][d]), plo(V[8*G+6][d], V[8*G+7][d])}; \
    const v4u bo_ = {phi(V[8*G+0][d], V[8*G+1][d]), phi(V[8*G+2][d], V[8*G+3][d]), phi(V[8*G+4][d], V[8*G+5][d]), phi(V[8*G+6][d], V[8*G+7][d])}; \
    acc[2*d] = mfma(PA, be_, acc[2*d]); acc[2*d+1] = mfma(PA, bo_, acc[2*d+1]); } } while (0)
#pragma unroll
    for (int i = VPRE + 2; i < PVPOS; ++i) { WAITV(); LDV(i); pin(); }
    PVGROUP(0, pa0);
    pin();
#pragma unroll
    for (int i = PVPOS; i < 16; ++i) { WAITV(); LDV(i); pin(); }
    signal(L, C_V, lane);
    STAMP(6);
    unsigned bt[8][4];
#pragma unroll
    for (int p = 0; p < 4; ++p) {
#pragma unroll
      for (int d = 0; d < 4; ++d) { bt[2 * d][p] = plo(V[8 + 2 * p][d], V[9 + 2 * p][d]); bt[2 * d + 1][p] = phi(V[8 + 2 * p][d], V[9 + 2 * p][d]); }
      pin();
    }
    STAMP(7);
#pragma unroll
    for (int e = 0; e < 8; ++e) acc[e] = mfma(pa1, v4u{bt[e][0], bt[e][1], bt[e][2], bt[e][3]}, acc[e]);
    {
      LAS f32x4* p = (LAS f32x4*)(L + L_OR + dw * 2048 + lane * 32);
      p[0] = f32x4{acc[0][0], acc[1][0], acc[2][0], acc[3][0]};
      p[1] = f32x4{acc[4][0], acc[5][0], acc[6][0], acc[7][0]};
    }
    STAMP(8);
    wg_barrier();
    STAMP_OUT();
    return;
  }

  const int c = w, s4 = c & 3;
  const rsrc_t rws = mkrs(ws, WS_END), rx = mkrs(x, 8 * 512 * 2);
  const rsrc_t rwo = mkrs(wo, 4096 * 512 * 2), rw2 = mkrs(w2, 1792 * 512 * 2);
  const int cc = kvh * 4 + j;
  const int vwo = (lane >> 3) * 1024 + (lane & 7) * 16, lrow = (lane >> 3) * W_LD + (lane & 7) * 16;
  v4u wa0, wa1, wb0;
  if (tid < 16) *(LAS unsigned*)(L + L_CNT + 4 * tid) = 0u;
  unsigned tag;
  if (heavy) {
    const int ld = do_q ? 8192 : 3584;
    const rsrc_t rwb = mkrs(do_q ? wq : w1, 512 * ld);
    const v4u xr = __builtin_amdgcn_raw_buffer_load_b128(rx, vv, c * 1024, 0);
    const int vb = (lane >> 2) * ld + (lane & 3) * 16, sb = c * 64 * ld + (do_q ? kvh * 1024 + r * 64 : kvh * 448 + (r - 16) * 64);
    v4u wt[4];
#pragma unroll
    for (int i = 0; i < 4; ++i) wt[i] = ldw(rwb, vb, sb + i * 16 * ld);
    pin();
    wg_barrier();
    tag = *(const __attribute__((address_space(4))) unsigned*)(ws + WS_EPOCH) + 1u;
    float xv[8];
#pragma unroll
    for (int k = 0; k < 4; ++k) { xv[2 * k] = lo16(xr[k]); xv[2 * k + 1] = hi16(xr[k]); }
    const float mean = wavesum(((xv[0] + xv[1]) + (xv[2] + xv[3])) + ((xv[4] + xv[5]) + (xv[6] + xv[7]))) * (1.0f / 512.0f);
    float q = 0.f;
#pragma unroll
    for (int k = 0; k < 8; ++k) { const float d = xv[k] - mean; q = fmaf(d, d, q); }
    const float var = rbf(wavesum(q) * (1.0f / 512.0f)), mb = rbf(mean);
    const float rden = __builtin_amdgcn_rcpf(rbf(__builtin_amdgcn_sqrtf(rbf(var + 1e-5f))));
    v4u xa;
#pragma unroll
    for (int k = 0; k < 4; ++k) xa[k] = pk(rbf(xv[2 * k] - mb) * rden, rbf(xv[2 * k + 1] - mb) * rden);
    *(LAS v4u*)(L + L_XN + c * XN_LD + vv) = xa;
    signal(L, C_XN, lane);
    STAMP(3);
#pragma unroll
    for (int i = 0; i < 4; ++i) *(LAS v4u*)(L + L_WQ + (c * 64 + i * 16 + (lane >> 2)) * S_LD + (lane & 3) * 16) = wt[i];
    await<1>(L, C_XN, 8);
    const f32x4 d0 = tile_mm<2, S_LD, XN_LD>(L + L_WQ + c * 64 * S_LD, L + L_XN + c * 128, lane);
    const f32x4 d1 = tile_mm<2, S_LD, XN_LD>(L + L_WQ + c * 64 * S_LD + 32, L + L_XN + c * 128, lane);
    *(LAS f32x4*)(L + L_RD + c * 1024 + (lane & 15) * 64 + g * 16) = (lane & 8) ? d1 : d0;
    signal(L, C_RD, lane);
    STAMP(4);
    if (c == 7) {
      await<1>(L, C_RD, 8);
      f32x4 d = *(LAS f32x4*)(L + L_RD + vv);
#pragma unroll
      for (int k = 1; k < 8; ++k) d += *(LAS f32x4*)(L + L_RD + k * 1024 + vv);
      const float fl = do_q ? -__builtin_inff() : 0.f;
      stg(rws, vv, do_q ? WS_QG + (kvh * 16 + r) * 1024 : WS_HG + (kvh * 7 + r - 16) * 1024,
          pk(fmaxf(d[0], fl), fmaxf(d[1], fl)), pk(fmaxf(d[2], fl), fmaxf(d[3], fl)), tag);
    }
    STAMP(5);
  }
  wb0 = v4u{0u, 0u, 0u, 0u};
  if (c < 7) wb0 = ldw(rw2, vwo, (cc * 56 + c * 8) * 1024 + b * 128);
  const bool wo_late = WO_LATE == 1 || (WO_LATE == 2 && heavy);
  if (!wo_late) {
    wa0 = ldw(rwo, vwo, (cc * 128 + c * 16) * 1024 + b * 128);
    wa1 = ldw(rwo, vwo, (cc * 128 + c * 16 + 8) * 1024 + b * 128);
  }
  pin();
  if (!heavy) {
    wg_barrier();
    tag = *(const __attribute__((address_space(4))) unsigned*)(ws + WS_EPOCH) + 1u;
  }
  const int lwo = L_WO + lrow;
  *(LAS v4u*)(L + lwo + (128 + c * 8) * W_LD) = wb0;
  if (!wo_late) {
    *(LAS v4u*)(L + lwo + (c * 16) * W_LD) = wa0;
    *(LAS v4u*)(L + lwo + (c * 16 + 8) * W_LD) = wa1;
  }
  STAMP(6);
  unsigned xo = 0;
  if (c == 2) {
    await<4>(L, C_K, 1);
    v4u a0, a1;
    const int so = WS_QG + kvh * 16384 + b * 64;
    gather2<8>(a0, a1, rws, (lane >> 2) * 512 + (lane & 3) * 16, so, so + 8192, tag);
    *(LAS v2u*)(L + L_Q + lane * 8) = v2u{a0.x, a0.z};
    *(LAS v2u*)(L + L_Q + 512 + lane * 8) = v2u{a1.x, a1.z};
    signal(L, C_Q, lane);
  } else if (c == 3) {
    await<8>(L, C_ML, 1);
    v4u a0, a1;
    const int u = 56 * cc + 4 * min(lane & 15, 13), so = WS_HG;
    gather2<8>(a0, a1, rws, (u >> 4) * 512 + g * 64 + (u & 15) * 4, so, so + 256, tag);
    const bool pad = (lane & 15) >= 14;
    *(LAS v2u*)(L + L_A + g * A_LD + 256 + (lane & 15) * 8) = pad ? v2u{0u, 0u} : v2u{a0.x, a0.z};
    *(LAS v2u*)(L + L_A + (g + 4) * A_LD + 256 + (lane & 15) * 8) = pad ? v2u{0u, 0u} : v2u{a1.x, a1.z};
  } else if (c == 1) {
    await<4>(L, C_ML, 8);
    const f32x2 ml = *(LAS f32x2*)(L + L_ML + (lane & 31) * 8);
    float M = ml[0];
    M = fmaxf(M, dpp<XOR1>(M)); M = fmaxf(M, dpp<XOR2>(M)); M = fmaxf(M, dpp<HMIR>(M));
    float Ls = ex2((ml[0] - M) * LOG2E) * ml[1];
    Ls += dpp<XOR1>(Ls); Ls += dpp<XOR2>(Ls); Ls += dpp<HMIR>(Ls);
    const int sml = WS_ML + (b * 8 + kvh) * 512;
    if ((lane & 0x27) == 0) stg(rws, lane * 2, sml + j * 128, asu(M), asu(Ls), tag);
    __builtin_amdgcn_s_sleep(32);
    v4u a0, a1;
    gather2<8>(a0, a1, rws, (lane & 1) * 128 + ((lane >> 3) & 3) * 16, sml, sml + 256, tag);
    float Mg = fmaxf(asf(a0.x), asf(a1.x));
    Mg = fmaxf(Mg, dpp<XOR1>(Mg));
    float Lg = ex2((asf(a0.x) - Mg) * LOG2E) * asf(a0.z) + ex2((asf(a1.x) - Mg) * LOG2E) * asf(a1.z);
    Lg += dpp<XOR1>(Lg);
    if (lane < 32) *(LAS float*)(L + L_SC + lane * 4) = ex2((ml[0] - Mg) * LOG2E) * __builtin_amdgcn_rcpf(Lg);
  } else if (c == 0) {
    xo = __builtin_amdgcn_raw_buffer_load_b16(rx, ((lane & 3) * 4 + (lane >> 4)) * 2, b * 1024 + cc * 32, 0);
  }
  if (wo_late) {
    await<4>(L, C_V, WO_CNT);
    wa0 = ldw(rwo, vwo, (cc * 128 + c * 16) * 1024 + b * 128);
    wa1 = ldw(rwo, vwo, (cc * 128 + c * 16 + 8) * 1024 + b * 128);
  }
  pin();
  STAMP(8);
  wg_barrier();
  STAMP(9);
  if (c < 4) {
    const f32x4 s0 = *(LAS f32x4*)(L + L_SC + c * 32), s1 = *(LAS f32x4*)(L + L_SC + c * 32 + 16);
    f32x2 o = {0.f, 0.f};
#pragma unroll
    for (int k = 0; k < 8; ++k) o += *(LAS f32x2*)(L + L_OR + k * 2048 + c * 512 + lane * 8) * (k < 4 ? s0[k] : s1[k - 4]);
    __builtin_amdgcn_raw_buffer_store_b64(v2u{pk(o[0], o[1]), tag}, rws, lane * 8, WS_OG + ((kvh * 8 + b) * 4 + j) * 2048 + c * 512, AUX_ST);
  }
  if (wo_late) {
    *(LAS v4u*)(L + lwo + (c * 16) * W_LD) = wa0;
    *(LAS v4u*)(L + lwo + (c * 16 + 8) * W_LD) = wa1;
  }
  STAMP(10);
  {
    __builtin_amdgcn_s_sleep(OPRE);
    v4u a0, a1;
    const int so = WS_OG + (kvh * 8 + c) * 8192 + j * 512;
    gather2<OSLEEP>(a0, a1, rws, (lane >> 5) * 2048 + (lane & 31) * 16, so, so + 4096, tag);
    const float e0 = tsum32(lo16(a0.x) + lo16(a1.x), lo16(a0.z) + lo16(a1.z)), e1 = tsum32(hi16(a0.x) + hi16(a1.x), hi16(a0.z) + hi16(a1.z));
    *(LAS unsigned*)(L + L_A + c * A_LD + (lane & 31) * 8 + (lane >> 5) * 4) = pk(e0, e1);
  }
  STAMP(11);
  wg_barrier();
  STAMP(12);
  if (c >= 4) {
    const f32x4 d = tile_mm<6, W_LD, A_LD>(L + L_WO + s4 * 32, L + L_A, lane);
    if (!(lane & 8)) stg(rws, (lane & 7) * 64 + g * 16, WS_YG + (b * 32 + cc) * 2048 + s4 * 512, pk(d[0], d[1]), pk(d[2], d[3]), tag);
  }
  STAMP(13);
  if (c == 0) {
    __builtin_amdgcn_s_sleep(YPRE);
    v4u a0, a1;
    const int so = WS_YG + kvh * 65536 + j * 512 + b * 64;
    gather2<YSLEEP>(a0, a1, rws, (lane >> 2) * 2048 + (lane & 3) * 16, so, so + 32768, tag);
    const float t0 = tsum32(lo16(a0.x) + lo16(a1.x), lo16(a0.z) + lo16(a1.z)), t1 = tsum32(hi16(a0.x) + hi16(a1.x), hi16(a0.z) + hi16(a1.z));
    float t = tsum16(t0, t1);
    t += dpp<ROR4>(t); t += dpp<ROR8>(t);
    if (!(lane & 12)) *(__bf16*)(out + b * 1024 + cc * 32 + ((lane & 3) * 4 + (lane >> 4)) * 2) = (__bf16)(lo16(xo) + t);
    if (bid == 0 && lane == 0) __builtin_amdgcn_raw_buffer_store_b32(tag, rws, 0, WS_EPOCH, 16);
    STAMP(14);
  }
  STAMP_OUT();
}

extern "C" void kernel_launch(void* const* d_in, const int* in_sizes, int n_in, void* d_out, int out_size, void* d_ws, size_t ws_size, hipStream_t stream) {
  fused_decode<<<256, 1024, 0, stream>>>((const unsigned char*)d_in[0], (const unsigned char*)d_in[1], (const unsigned char*)d_in[2],
                                          (const unsigned char*)d_in[3], (const unsigned char*)d_in[4], (const unsigned char*)d_in[6],
                                          (const unsigned char*)d_in[7], (unsigned char*)d_out, (unsigned char*)d_ws + WS_BASE);
}
```

```cpp
#define PROBE 99
#define PCLS 0
#define PNEG 0
#include <hip/hip_runtime.h>
#include <hip/hip_bf16.h>
#include <stdint.h>

#define CHL 4
#define CHH 4
#define QTRIG 8
#define PVPOS 12
#define KPRE 2
#define WPRE 8
#define W0 4
#define N0 0
#define VPRE 4
#define WO_LATE 2
#define WO_CNT 4
#define OSLEEP 8
#define YSLEEP 8
#define OPRE 8
#define YPRE 16
#define AUX_KV 2
#define AUX_W 2
#define AUX_ST 16
#define AUX_LD 16
#define WS_BASE 0

typedef unsigned v4u __attribute__((ext_vector_type(4)));
typedef unsigned v2u __attribute__((ext_vector_type(2)));
typedef float f32x4 __attribute__((ext_vector_type(4)));
typedef float f32x2 __attribute__((ext_vector_type(2)));
typedef short v4s __attribute__((ext_vector_type(4)));
typedef __bf16 bf16x8 __attribute__((ext_vector_type(8)));
typedef __bf16 bf16x2 __attribute__((ext_vector_type(2)));
#define LAS __attribute__((address_space(3)))
typedef __amdgpu_buffer_rsrc_t rsrc_t;

constexpr int SK = 8192, WIN = 2048;
constexpr unsigned SPIN_CAP = 1u << 16;
constexpr float LOG2E = 1.4426950408889634f;
constexpr int WS_EPOCH = 0;
constexpr int WS_QG = 4096;
constexpr int WS_HG = WS_QG + 256 * 512;
constexpr int WS_ML = WS_HG + 128 * 512;
constexpr int WS_OG = WS_ML + 64 * 512;
constexpr int WS_YG = WS_OG + 256 * 2048;
constexpr int WS_END = WS_YG + 256 * 2048;
#define RT32_(v) do { unsigned long long t_; asm volatile("s_memrealtime %0\n\ts_waitcnt lgkmcnt(0)" : "=s"(t_) :: "memory"); v = (unsigned)t_; } while (0)
#define STAMP(k) do { if ((k) == 0) RT32_(pt0); else if ((k) == PROBE && (w >= 8) == (PCLS == 0)) { __builtin_amdgcn_sched_barrier(0); unsigned t1_; RT32_(t1_); \
    if (lane == 0) __hip_atomic_fetch_max((LAS unsigned*)(L + L_CNT + 60), t1_ - pt0, __ATOMIC_RELAXED, __HIP_MEMORY_SCOPE_WORKGROUP); __builtin_amdgcn_sched_barrier(0); } } while (0)
#define STAMP_OUT() do { if (w == 0) { unsigned q_ = __builtin_amdgcn_readfirstlane(__hip_atomic_load((LAS unsigned*)(L + L_CNT + 60), __ATOMIC_RELAXED, __HIP_MEMORY_SCOPE_WORKGROUP)); \
    if (PNEG) q_ = 3000u - q_; q_ = q_ > 6000u ? 6000u : q_; for (int it_ = 0; it_ < 1000000; ++it_) { unsigned t1_; RT32_(t1_); if (t1_ - pt0 >= 5000u + 10u * q_) break; __builtin_amdgcn_s_sleep(1); } } } while (0)
constexpr int XN_LD = 1040;
constexpr int L_XN = 0;
constexpr int W_LD = 144;
constexpr int S_LD = 80;
constexpr int L_WQ = 8448;
constexpr int L_RD = L_WQ + 512 * S_LD;
constexpr int L_WO = L_RD + 8192;
constexpr int A_LD = 400;
constexpr int L_A = L_WO + 192 * W_LD;
constexpr int L_Q = L_A + 8 * A_LD;
constexpr int L_ML = L_Q + 1024;
constexpr int L_SC = L_ML + 256;
constexpr int L_CNT = L_SC + 128;
constexpr int L_OR = L_CNT + 64;
constexpr int L_END = L_OR + 16384;
constexpr int LDS_BYTES = 112 * 1024;
static_assert(L_END <= LDS_BYTES && L_A % 16 == 0 && L_Q % 16 == 0 && L_OR % 16 == 0, "lds");
enum { C_XN = 0, C_Q = 1, C_ML = 2, C_V = 3, C_K = 4, C_RD = 5 };

__device__ __forceinline__ rsrc_t mkrs(const void* p, int bytes) { return __builtin_amdgcn_make_buffer_rsrc((void*)p, 0, bytes, 0x00020000); }
template <int N> __device__ __forceinline__ void waitvm() { asm volatile("s_waitcnt vmcnt(%0)" ::"n"(N) : "memory"); }
__device__ __forceinline__ void wg_barrier() { asm volatile("s_waitcnt lgkmcnt(0)\n\ts_barrier" ::: "memory"); }
__device__ __forceinline__ void pin() { __builtin_amdgcn_sched_barrier(0); }
__device__ __forceinline__ void reload() { asm volatile("" ::: "memory"); }
__device__ __forceinline__ float asf(unsigned u) { return __builtin_bit_cast(float, u); }
__device__ __forceinline__ unsigned asu(float f) { return __builtin_bit_cast(unsigned, f); }
__device__ __forceinline__ float lo16(unsigned u) { return asf(u << 16); }
__device__ __forceinline__ float hi16(unsigned u) { return asf(u & 0xffff0000u); }
__device__ __forceinline__ float rbf(float f) { return (float)(__bf16)f; }
__device__ __forceinline__ unsigned pk(float a, float b) { bf16x2 v = {(__bf16)a, (__bf16)b}; return __builtin_bit_cast(unsigned, v); }
template <int CTRL> __device__ __forceinline__ float dpp(float x) { return asf(__builtin_amdgcn_mov_dpp(asu(x), CTRL, 0xf, 0xf, true)); }
constexpr int XOR1 = 0xB1, XOR2 = 0x4E, HMIR = 0x141, ROR4 = 0x124, ROR8 = 0x128;
__device__ __forceinline__ float sum16(float x) { auto s = __builtin_amdgcn_permlane16_swap(asu(x), asu(x), false, false); return asf(s[0]) + asf(s[1]); }
__device__ __forceinline__ float sum32(float x) { auto s = __builtin_amdgcn_permlane32_swap(asu(x), asu(x), false, false); return asf(s[0]) + asf(s[1]); }
__device__ __forceinline__ float tsum32(float a, float b) { auto s = __builtin_amdgcn_permlane32_swap(asu(a), asu(b), false, false); return asf(s[0]) + asf(s[1]); }
__device__ __forceinline__ float tsum16(float a, float b) { auto s = __builtin_amdgcn_permlane16_swap(asu(a), asu(b), false, false); return asf(s[0]) + asf(s[1]); }
__device__ __forceinline__ float max16(float x) { auto s = __builtin_amdgcn_permlane16_swap(asu(x), asu(x), false, false); return fmaxf(asf(s[0]), asf(s[1])); }
__device__ __forceinline__ float max32(float x) { auto s = __builtin_amdgcn_permlane32_swap(asu(x), asu(x), false, false); return fmaxf(asf(s[0]), asf(s[1])); }
__device__ __forceinline__ float rowsum(float x) { x += dpp<XOR1>(x); x += dpp<XOR2>(x); x += dpp<ROR4>(x); x += dpp<ROR8>(x); return x; }
__device__ __forceinline__ float wavesum(float x) { return sum32(sum16(rowsum(x))); }
__device__ __forceinline__ float ex2(float x) { return __builtin_amdgcn_exp2f(x); }
__device__ __forceinline__ f32x4 mfma(v4u a, v4u b, f32x4 c) { return __builtin_amdgcn_mfma_f32_16x16x32_bf16(__builtin_bit_cast(bf16x8, a), __builtin_bit_cast(bf16x8, b), c, 0, 0, 0); }
__device__ __forceinline__ unsigned plo(unsigned a, unsigned b) { return __builtin_amdgcn_perm(b, a, 0x05040100u); }
__device__ __forceinline__ unsigned phi(unsigned a, unsigned b) { return __builtin_amdgcn_perm(b, a, 0x07060302u); }
__device__ __forceinline__ bool tagok(v4u v, unsigned tag) { return (v.y == tag) & (v.w == tag); }
__device__ __forceinline__ v4u ldw(rsrc_t r, int vo, int so) { return __builtin_amdgcn_raw_buffer_load_b128(r, vo, so, AUX_W); }
__device__ __forceinline__ v4u ldg(rsrc_t r, int vo, int so) { return __builtin_amdgcn_raw_buffer_load_b128(r, vo, so, AUX_LD); }
__device__ __forceinline__ void stg(rsrc_t r, int vo, int so, unsigned a, unsigned b, unsigned tag) { __builtin_amdgcn_raw_buffer_store_b128(v4u{a, tag, b, tag}, r, vo, so, AUX_ST); }
__device__ __forceinline__ void signal(LAS unsigned char* L, int k, int lane) {
  asm volatile("s_waitcnt lgkmcnt(0)" ::: "memory");
  if (lane == 0) __hip_atomic_fetch_add((LAS unsigned*)(L + L_CNT + 4 * k), 1u, __ATOMIC_RELAXED, __HIP_MEMORY_SCOPE_WORKGROUP);
}
template <int SLP> __device__ __forceinline__ void await(LAS unsigned char* L, int k, unsigned n) {
#pragma unroll 1
  for (unsigned spin = 0; spin < (SPIN_CAP << 4); ++spin) {
    if ((unsigned)__builtin_amdgcn_readfirstlane(__hip_atomic_load((LAS unsigned*)(L + L_CNT + 4 * k), __ATOMIC_RELAXED, __HIP_MEMORY_SCOPE_WORKGROUP)) >= n) break;
    __builtin_amdgcn_s_sleep(SLP);
  }
  reload();
}
template <int NS, int WLD, int ALD> __device__ __forceinline__ f32x4 tile_mm(LAS unsigned char* wt, LAS unsigned char* act, int lane) {
  f32x4 d = {0.f, 0.f, 0.f, 0.f};
  LAS unsigned char* wp = wt + ((lane >> 4) * 8 + ((lane >> 2) & 3)) * WLD + (lane & 3) * 8;
  LAS unsigned char* ap = act + (lane & 7) * ALD + (lane >> 4) * 16;
#pragma unroll
  for (int s = 0; s < NS; ++s) {
    const v2u lo = __builtin_bit_cast(v2u, __builtin_amdgcn_ds_read_tr16_b64_v4i16((LAS v4s*)(wp + s * 32 * WLD)));
    const v2u hi = __builtin_bit_cast(v2u, __builtin_amdgcn_ds_read_tr16_b64_v4i16((LAS v4s*)(wp + (s * 32 + 4) * WLD)));
    d = mfma(v4u{lo.x, lo.y, hi.x, hi.y}, *(LAS v4u*)(ap + s * 64), d);
  }
  return d;
}
template <int SLP> __device__ __forceinline__ void gather2(v4u& a0, v4u& a1, rsrc_t rws, int vo, int so0, int so1, unsigned tag) {
#pragma unroll 1
  for (unsigned spin = 0; spin < SPIN_CAP; ++spin) {
    reload();
    a0 = ldg(rws, vo, so0); a1 = ldg(rws, vo, so1);
    if (__all(tagok(a0, tag) & tagok(a1, tag))) break;
    __builtin_amdgcn_s_sleep(SLP);
  }
}

__global__ void __launch_bounds__(1024, 4)
fused_decode(const unsigned char* __restrict__ x, const unsigned char* __restrict__ kc, const unsigned char* __restrict__ vc,
             const unsigned char* __restrict__ wq, const unsigned char* __restrict__ wo, const unsigned char* __restrict__ w1,
             const unsigned char* __restrict__ w2, unsigned char* out, unsigned char* ws) {
  __shared__ __attribute__((aligned(16))) unsigned char smem[LDS_BYTES];
  LAS unsigned char* L = (LAS unsigned char*)smem;
  const int bid = blockIdx.x, kvh = bid & 7, r = bid >> 3, b = r & 7, j = r >> 3;
  const int tid = threadIdx.x, lane = tid & 63, w = __builtin_amdgcn_readfirstlane(tid >> 6);
  const int g = lane >> 4, vv = lane * 16;
  const bool do_q = r < 16, heavy = r < 23;
  unsigned pt0 = 0;
  STAMP(0);
  asm volatile("" ::"s"(x), "s"(kc), "s"(vc), "s"(wq), "s"(wo), "s"(w1), "s"(w2), "s"(out), "s"(ws));
  STAMP(1);

  if (w >= 8) {
    const int dw = w - 8;
    const size_t kvoff = ((size_t)(b * 8 + kvh) * SK + (SK - WIN) + j * 512) * 256;
    const rsrc_t rk = mkrs(kc + kvoff, 131072), rv = mkrs(vc + kvoff, 131072);
    const int rot = (int)(((unsigned)bid * 2654435761u) >> 20) + dw;
    const int vk = ((lane >> 2) & 3) * 256 + g * 64 + (lane & 3) * 16;
    v4u K[16], V[16];
#define PIECE(i) ((((i) * 8 + rot) & 127) << 10)
#define LDK(i) K[i] = __builtin_amdgcn_raw_buffer_load_b128(rk, vk, PIECE(i), AUX_KV)
#define LDV(i) V[i] = __builtin_amdgcn_raw_buffer_load_b128(rv, vv, PIECE(i), AUX_KV)
#define WAITV() do { if (heavy) waitvm<CHH - 1>(); else waitvm<CHL - 1>(); } while (0)
#pragma unroll
    for (int i = 0; i < KPRE; ++i) { LDK(i); pin(); }
    __builtin_amdgcn_s_barrier();
    STAMP(9);
#pragma unroll
    for (int i = KPRE; i < 16; ++i) { if (i >= W0 && i < N0) { if (!heavy) waitvm<W0 - 1>(); } if (i >= CHL) WAITV(); if (i == CHH) STAMP(10); if (i == 8) STAMP(11); if (i == 12) STAMP(12); LDK(i); if (i == QTRIG) signal(L, C_K, lane); pin(); }
    STAMP(2);
#pragma unroll
    for (int i = 0; i < VPRE; ++i) { WAITV(); LDV(i); pin(); }
    STAMP(3);
    await<1>(L, C_Q, 1);
    STAMP(4);
    const v4u qb = *(LAS v4u*)(L + L_Q + vk);
    float s[16];
#pragma unroll
    for (int i4 = 0; i4 < 4; ++i4) {
#pragma unroll
      for (int i = 4 * i4; i < 4 * i4 + 4; i += 2) {
        const f32x4 d = mfma(K[i], qb, f32x4{0.f, 0.f, 0.f, 0.f}), e = mfma(K[i + 1], qb, f32x4{0.f, 0.f, 0.f, 0.f});
        const float v0 = (dpp<0x00>(d[0]) + dpp<0x55>(d[1])) + (dpp<0xAA>(d[2]) + dpp<0xFF>(d[3]));
        const float v1 = (dpp<0x00>(e[0]) + dpp<0x55>(e[1])) + (dpp<0xAA>(e[2]) + dpp<0xFF>(e[3]));
        const unsigned u = pk(v0, v1);
        const unsigned t = pk(lo16(u) * 0.088388347648318440f, hi16(u) * 0.088388347648318440f);
        s[i] = lo16(t); s[i + 1] = hi16(t);
      }
      pin();
      if (i4 & 1) { LDV(VPRE + (i4 >> 1)); pin(); }
    }
    float mw = fmaxf(s[0], s[1]);
#pragma unroll
    for (int i = 2; i < 16; i += 2) mw = fmaxf(fmaxf(mw, s[i]), s[i + 1]);
    mw = max32(max16(mw));
    float lw = 0.f;
    {
      const float mc = -mw * LOG2E;
#pragma unroll
      for (int i = 0; i < 16; ++i) { s[i] = ex2(fmaf(s[i], LOG2E, mc)); lw += s[i]; }
    }
    lw = sum32(sum16(lw));
    const v4u pa0 = {pk(s[0], s[1]), pk(s[2], s[3]), pk(s[4], s[5]), pk(s[6], s[7])};
    const v4u pa1 = {pk(s[8], s[9]), pk(s[10], s[11]), pk(s[12], s[13]), pk(s[14], s[15])};
    if ((lane & 0x33) == 0) *(LAS f32x2*)(L + L_ML + lane * 16 + dw * 8) = f32x2{mw, lw};
    signal(L, C_ML, lane);
    STAMP(5);
    pin();
    f32x4 acc[8];
#pragma unroll
    for (int e = 0; e < 8; ++e) acc[e] = f32x4{0.f, 0.f, 0.f, 0.f};
#define PVGROUP(G, PA) do { _Pragma("unroll") for (int d = 0; d < 4; ++d) { \
    const v4u be_ = {plo(V[8*G+0][d], V[8*G+1][d]), plo(V[8*G+2][d], V[8*G+3][d]), plo(V[8*G+4][d], V[8*G+5][d]), plo(V[8*G+6][d], V[8*G+7][d])}; \
    const v4u bo_ = {phi(V[8*G+0][d], V[8*G+1][d]), phi(V[8*G+2][d], V[8*G+3][d]), phi(V[8*G+4][d], V[8*G+5][d]), phi(V[8*G+6][d], V[8*G+7][d])}; \
    acc[2*d] = mfma(PA, be_, acc[2*d]); acc[2*d+1] = mfma(PA, bo_, acc[2*d+1]); } } while (0)
#pragma unroll
    for (int i = VPRE + 2; i < PVPOS; ++i) { WAITV(); LDV(i); pin(); }
    PVGROUP(0, pa0);
    pin();
#pragma unroll
    for (int i = PVPOS; i < 16; ++i) { WAITV(); LDV(i); pin(); }
    signal(L, C_V, lane);
    STAMP(6);
    unsigned bt[8][4];
#pragma unroll
    for (int p = 0; p < 4; ++p) {
#pragma unroll
      for (int d = 0; d < 4; ++d) { bt[2 * d][p] = plo(V[8 + 2 * p][d], V[9 + 2 * p][d]); bt[2 * d + 1][p] = phi(V[8 + 2 * p][d], V[9 + 2 * p][d]); }
      pin();
    }
    STAMP(7);
#pragma unroll
    for (int e = 0; e < 8; ++e) acc[e] = mfma(pa1, v4u{bt[e][0], bt[e][1], bt[e][2], bt[e][3]}, acc[e]);
    {
      LAS f32x4* p = (LAS f32x4*)(L + L_OR + dw * 2048 + lane * 32);
      p[0] = f32x4{acc[0][0], acc[1][0], acc[2][0], acc[3][0]};
      p[1] = f32x4{acc[4][0], acc[5][0], acc[6][0], acc[7][0]};
    }
    STAMP(8);
    wg_barrier();
    STAMP_OUT();
    return;
  }

  const int c = w, s4 = c & 3;
  const rsrc_t rws = mkrs(ws, WS_END), rx = mkrs(x, 8 * 512 * 2);
  const rsrc_t rwo = mkrs(wo, 4096 * 512 * 2), rw2 = mkrs(w2, 1792 * 512 * 2);
  const int cc = kvh * 4 + j;
  const int vwo = (lane >> 3) * 1024 + (lane & 7) * 16, lrow = (lane >> 3) * W_LD + (lane & 7) * 16;
  v4u wa0, wa1, wb0;
  if (tid < 16) *(LAS unsigned*)(L + L_CNT + 4 * tid) = 0u;
  unsigned tag;
  if (heavy) {
    const int ld = do_q ? 8192 : 3584;
    const rsrc_t rwb = mkrs(do_q ? wq : w1, 512 * ld);
    const v4u xr = __builtin_amdgcn_raw_buffer_load_b128(rx, vv, c * 1024, 0);
    const int vb = (lane >> 2) * ld + (lane & 3) * 16, sb = c * 64 * ld + (do_q ? kvh * 1024 + r * 64 : kvh * 448 + (r - 16) * 64);
    v4u wt[4];
#pragma unroll
    for (int i = 0; i < 4; ++i) wt[i] = ldw(rwb, vb, sb + i * 16 * ld);
    pin();
    wg_barrier();
    tag = *(const __attribute__((address_space(4))) unsigned*)(ws + WS_EPOCH) + 1u;
    float xv[8];
#pragma unroll
    for (int k = 0; k < 4; ++k) { xv[2 * k] = lo16(xr[k]); xv[2 * k + 1] = hi16(xr[k]); }
    const float mean = wavesum(((xv[0] + xv[1]) + (xv[2] + xv[3])) + ((xv[4] + xv[5]) + (xv[6] + xv[7]))) * (1.0f / 512.0f);
    float q = 0.f;
#pragma unroll
    for (int k = 0; k < 8; ++k) { const float d = xv[k] - mean; q = fmaf(d, d, q); }
    const float var = rbf(wavesum(q) * (1.0f / 512.0f)), mb = rbf(mean);
    const float rden = __builtin_amdgcn_rcpf(rbf(__builtin_amdgcn_sqrtf(rbf(var + 1e-5f))));
    v4u xa;
#pragma unroll
    for (int k = 0; k < 4; ++k) xa[k] = pk(rbf(xv[2 * k] - mb) * rden, rbf(xv[2 * k + 1] - mb) * rden);
    *(LAS v4u*)(L + L_XN + c * XN_LD + vv) = xa;
    signal(L, C_XN, lane);
    STAMP(3);
#pragma unroll
    for (int i = 0; i < 4; ++i) *(LAS v4u*)(L + L_WQ + (c * 64 + i * 16 + (lane >> 2)) * S_LD + (lane & 3) * 16) = wt[i];
    await<1>(L, C_XN, 8);
    const f32x4 d0 = tile_mm<2, S_LD, XN_LD>(L + L_WQ + c * 64 * S_LD, L + L_XN + c * 128, lane);
    const f32x4 d1 = tile_mm<2, S_LD, XN_LD>(L + L_WQ + c * 64 * S_LD + 32, L + L_XN + c * 128, lane);
    *(LAS f32x4*)(L + L_RD + c * 1024 + (lane & 15) * 64 + g * 16) = (lane & 8) ? d1 : d0;
    signal(L, C_RD, lane);
    STAMP(4);
    if (c == 7) {
      await<1>(L, C_RD, 8);
      f32x4 d = *(LAS f32x4*)(L + L_RD + vv);
#pragma unroll
      for (int k = 1; k < 8; ++k) d += *(LAS f32x4*)(L + L_RD + k * 1024 + vv);
      const float fl = do_q ? -__builtin_inff() : 0.f;
      stg(rws, vv, do_q ? WS_QG + (kvh * 16 + r) * 1024 : WS_HG + (kvh * 7 + r - 16) * 1024,
          pk(fmaxf(d[0], fl), fmaxf(d[1], fl)), pk(fmaxf(d[2], fl), fmaxf(d[3], fl)), tag);
    }
    STAMP(5);
  }
  wb0 = v4u{0u, 0u, 0u, 0u};
  if (c < 7) wb0 = ldw(rw2, vwo, (cc * 56 + c * 8) * 1024 + b * 128);
  const bool wo_late = WO_LATE == 1 || (WO_LATE == 2 && heavy);
  if (!wo_late) {
    wa0 = ldw(rwo, vwo, (cc * 128 + c * 16) * 1024 + b * 128);
    wa1 = ldw(rwo, vwo, (cc * 128 + c * 16 + 8) * 1024 + b * 128);
  }
  pin();
  if (!heavy) {
    wg_barrier();
    tag = *(const __attribute__((address_space(4))) unsigned*)(ws + WS_EPOCH) + 1u;
  }
  const int lwo = L_WO + lrow;
  *(LAS v4u*)(L + lwo + (128 + c * 8) * W_LD) = wb0;
  if (!wo_late) {
    *(LAS v4u*)(L + lwo + (c * 16) * W_LD) = wa0;
    *(LAS v4u*)(L + lwo + (c * 16 + 8) * W_LD) = wa1;
  }
  STAMP(6);
  unsigned xo = 0;
  if (c == 2) {
    await<4>(L, C_K, 1);
    v4u a0, a1;
    const int so = WS_QG + kvh * 16384 + b * 64;
    gather2<8>(a0, a1, rws, (lane >> 2) * 512 + (lane & 3) * 16, so, so + 8192, tag);
    *(LAS v2u*)(L + L_Q + lane * 8) = v2u{a0.x, a0.z};
    *(LAS v2u*)(L + L_Q + 512 + lane * 8) = v2u{a1.x, a1.z};
    signal(L, C_Q, lane);
  } else if (c == 3) {
    await<8>(L, C_ML, 1);
    v4u a0, a1;
    const int u = 56 * cc + 4 * min(lane & 15, 13), so = WS_HG;
    gather2<8>(a0, a1, rws, (u >> 4) * 512 + g * 64 + (u & 15) * 4, so, so + 256, tag);
    const bool pad = (lane & 15) >= 14;
    *(LAS v2u*)(L + L_A + g * A_LD + 256 + (lane & 15) * 8) = pad ? v2u{0u, 0u} : v2u{a0.x, a0.z};
    *(LAS v2u*)(L + L_A + (g + 4) * A_LD + 256 + (lane & 15) * 8) = pad ? v2u{0u, 0u} : v2u{a1.x, a1.z};
  } else if (c == 1) {
    await<4>(L, C_ML, 8);
    const f32x2 ml = *(LAS f32x2*)(L + L_ML + (lane & 31) * 8);
    float M = ml[0];
    M = fmaxf(M, dpp<XOR1>(M)); M = fmaxf(M, dpp<XOR2>(M)); M = fmaxf(M, dpp<HMIR>(M));
    float Ls = ex2((ml[0] - M) * LOG2E) * ml[1];
    Ls += dpp<XOR1>(Ls); Ls += dpp<XOR2>(Ls); Ls += dpp<HMIR>(Ls);
    const int sml = WS_ML + (b * 8 + kvh) * 512;
    if ((lane & 0x27) == 0) stg(rws, lane * 2, sml + j * 128, asu(M), asu(Ls), tag);
    __builtin_amdgcn_s_sleep(32);
    v4u a0, a1;
    gather2<8>(a0, a1, rws, (lane & 1) * 128 + ((lane >> 3) & 3) * 16, sml, sml + 256, tag);
    float Mg = fmaxf(asf(a0.x), asf(a1.x));
    Mg = fmaxf(Mg, dpp<XOR1>(Mg));
    float Lg = ex2((asf(a0.x) - Mg) * LOG2E) * asf(a0.z) + ex2((asf(a1.x) - Mg) * LOG2E) * asf(a1.z);
    Lg += dpp<XOR1>(Lg);
    if (lane < 32) *(LAS float*)(L + L_SC + lane * 4) = ex2((ml[0] - Mg) * LOG2E) * __builtin_amdgcn_rcpf(Lg);
  } else if (c == 0) {
    xo = __builtin_amdgcn_raw_buffer_load_b16(rx, ((lane & 3) * 4 + (lane >> 4)) * 2, b * 1024 + cc * 32, 0);
  }
  if (wo_late) {
    await<4>(L, C_V, WO_CNT);
    wa0 = ldw(rwo, vwo, (cc * 128 + c * 16) * 1024 + b * 128);
    wa1 = ldw(rwo, vwo, (cc * 128 + c * 16 + 8) * 1024 + b * 128);
  }
  pin();
  STAMP(8);
  wg_barrier();
  STAMP(9);
  if (c < 4) {
    const f32x4 s0 = *(LAS f32x4*)(L + L_SC + c * 32), s1 = *(LAS f32x4*)(L + L_SC + c * 32 + 16);
    f32x2 o = {0.f, 0.f};
#pragma unroll
    for (int k = 0; k < 8; ++k) o += *(LAS f32x2*)(L + L_OR + k * 2048 + c * 512 + lane * 8) * (k < 4 ? s0[k] : s1[k - 4]);
    __builtin_amdgcn_raw_buffer_store_b64(v2u{pk(o[0], o[1]), tag}, rws, lane * 8, WS_OG + ((kvh * 8 + b) * 4 + j) * 2048 + c * 512, AUX_ST);
  }
  if (wo_late) {
    *(LAS v4u*)(L + lwo + (c * 16) * W_LD) = wa0;
    *(LAS v4u*)(L + lwo + (c * 16 + 8) * W_LD) = wa1;
  }
  STAMP(10);
  {
    __builtin_amdgcn_s_sleep(OPRE);
    v4u a0, a1;
    const int so = WS_OG + (kvh * 8 + c) * 8192 + j * 512;
    gather2<OSLEEP>(a0, a1, rws, (lane >> 5) * 2048 + (lane & 31) * 16, so, so + 4096, tag);
    const float e0 = tsum32(lo16(a0.x) + lo16(a1.x), lo16(a0.z) + lo16(a1.z)), e1 = tsum32(hi16(a0.x) + hi16(a1.x), hi16(a0.z) + hi16(a1.z));
    *(LAS unsigned*)(L + L_A + c * A_LD + (lane & 31) * 8 + (lane >> 5) * 4) = pk(e0, e1);
  }
  STAMP(11);
  wg_barrier();
  STAMP(12);
  if (c >= 4) {
    const f32x4 d = tile_mm<6, W_LD, A_LD>(L + L_WO + s4 * 32, L + L_A, lane);
    if (!(lane & 8)) stg(rws, (lane & 7) * 64 + g * 16, WS_YG + (b * 32 + cc) * 2048 + s4 * 512, pk(d[0], d[1]), pk(d[2], d[3]), tag);
  }
  STAMP(13);
  if (c == 0) {
    __builtin_amdgcn_s_sleep(YPRE);
    v4u a0, a1;
    const int so = WS_YG + kvh * 65536 + j * 512 + b * 64;
    gather2<YSLEEP>(a0, a1, rws, (lane >> 2) * 2048 + (lane & 3) * 16, so, so + 32768, tag);
    const float t0 = tsum32(lo16(a0.x) + lo16(a1.x), lo16(a0.z) + lo16(a1.z)), t1 = tsum32(hi16(a0.x) + hi16(a1.x), hi16(a0.z) + hi16(a1.z));
    float t = tsum16(t0, t1);
    t += dpp<ROR4>(t); t += dpp<ROR8>(t);
    if (!(lane & 12)) *(__bf16*)(out + b * 1024 + cc * 32 + ((lane & 3) * 4 + (lane >> 4)) * 2) = (__bf16)(lo16(xo) + t);
    if (bid == 0 && lane == 0) __builtin_amdgcn_raw_buffer_store_b32(tag, rws, 0, WS_EPOCH, 16);
    STAMP(14);
  }
  STAMP_OUT();
}

extern "C" void kernel_launch(void* const* d_in, const int* in_sizes, int n_in, void* d_out, int out_size, void* d_ws, size_t ws_size, hipStream_t stream) {
  fused_decode<<<256, 1024, 0, stream>>>((const unsigned char*)d_in[0], (const unsigned char*)d_in[1], (const unsigned char*)d_in[2],
                                          (const unsigned char*)d_in[3], (const unsigned char*)d_in[4], (const unsigned char*)d_in[6],
                                          (const unsigned char*)d_in[7], (unsigned char*)d_out, (unsigned char*)d_ws + WS_BASE);
}
```

```cpp
#define PROBE 99
#define PCLS 0
#define PNEG 0
#include <hip/hip_runtime.h>
#include <hip/hip_bf16.h>
#include <stdint.h>

#define CHL 4
#define CHH 4
#define QTRIG 8
#define PVPOS 12
#define KPRE 2
#define WPRE 8
#define W0 4
#define N0 0
#define VPRE 4
#define WO_LATE 2
#define WO_CNT 4
#define OSLEEP 8
#define YSLEEP 8
#define OPRE 8
#define YPRE 16
#define AUX_KV 2
#define AUX_W 2
#define AUX_ST 16
#define AUX_LD 16
#define WS_BASE 0

typedef unsigned v4u __attribute__((ext_vector_type(4)));
typedef unsigned v2u __attribute__((ext_vector_type(2)));
typedef float f32x4 __attribute__((ext_vector_type(4)));
typedef float f32x2 __attribute__((ext_vector_type(2)));
typedef short v4s __attribute__((ext_vector_type(4)));
typedef __bf16 bf16x8 __attribute__((ext_vector_type(8)));
typedef __bf16 bf16x2 __attribute__((ext_vector_type(2)));
#define LAS __attribute__((address_space(3)))
typedef __amdgpu_buffer_rsrc_t rsrc_t;

constexpr int SK = 8192, WIN = 2048;
constexpr unsigned SPIN_CAP = 1u << 16;
constexpr float LOG2E = 1.4426950408889634f;
constexpr int WS_EPOCH = 0;
constexpr int WS_QG = 4096;
constexpr int WS_HG = WS_QG + 256 * 512;
constexpr int WS_ML = WS_HG + 128 * 512;
constexpr int WS_OG = WS_ML + 64 * 512;
constexpr int WS_YG = WS_OG + 256 * 2048;
constexpr int WS_END = WS_YG + 256 * 2048;
#define RT32_(v) do { unsigned long long t_; asm volatile("s_memrealtime %0\n\ts_waitcnt lgkmcnt(0)" : "=s"(t_) :: "memory"); v = (unsigned)t_; } while (0)
#define STAMP(k) do { if ((k) == 0) RT32_(pt0); else if ((k) == PROBE && (w >= 8) == (PCLS == 0)) { __builtin_amdgcn_sched_barrier(0); unsigned t1_; RT32_(t1_); \
    if (lane == 0) __hip_atomic_fetch_max((LAS unsigned*)(L + L_CNT + 60), t1_ - pt0, __ATOMIC_RELAXED, __HIP_MEMORY_SCOPE_WORKGROUP); __builtin_amdgcn_sched_barrier(0); } } while (0)
#define STAMP_OUT() do { if (w == 0) { unsigned q_ = __builtin_amdgcn_readfirstlane(__hip_atomic_load((LAS unsigned*)(L + L_CNT + 60), __ATOMIC_RELAXED, __HIP_MEMORY_SCOPE_WORKGROUP)); \
    if (PNEG) q_ = 3000u - q_; q_ = q_ > 6000u ? 6000u : q_; for (int it_ = 0; it_ < 1000000; ++it_) { unsigned t1_; RT32_(t1_); if (t1_ - pt0 >= 5000u + 10u * q_) break; __builtin_amdgcn_s_sleep(1); } } } while (0)
constexpr int XN_LD = 1040;
constexpr int L_XN = 0;
constexpr int W_LD = 144;
constexpr int S_LD = 80;
constexpr int L_WQ = 8448;
constexpr int L_RD = L_WQ + 512 * S_LD;
constexpr int L_WO = L_RD + 8192;
constexpr int A_LD = 400;
constexpr int L_A = L_WO + 192 * W_LD;
constexpr int L_Q = L_A + 8 * A_LD;
constexpr int L_ML = L_Q + 1024;
constexpr int L_SC = L_ML + 256;
constexpr int L_CNT = L_SC + 128;
constexpr int L_OR = L_CNT + 64;
constexpr int L_END = L_OR + 16384;
constexpr int LDS_BYTES = 112 * 1024;
static_assert(L_END <= LDS_BYTES && L_A % 16 == 0 && L_Q % 16 == 0 && L_OR % 16 == 0, "lds");
enum { C_XN = 0, C_Q = 1, C_ML = 2, C_V = 3, C_K = 4, C_RD = 5 };

__device__ __forceinline__ rsrc_t mkrs(const void* p, int bytes) { return __builtin_amdgcn_make_buffer_rsrc((void*)p, 0, bytes, 0x00020000); }
template <int N> __device__ __forceinline__ void waitvm() { asm volatile("s_waitcnt vmcnt(%0)" ::"n"(N) : "memory"); }
__device__ __forceinline__ void wg_barrier() { asm volatile("s_waitcnt lgkmcnt(0)\n\ts_barrier" ::: "memory"); }
__device__ __forceinline__ void pin() { __builtin_amdgcn_sched_barrier(0); }
__device__ __forceinline__ void reload() { asm volatile("" ::: "memory"); }
__device__ __forceinline__ float asf(unsigned u) { return __builtin_bit_cast(float, u); }
__device__ __forceinline__ unsigned asu(float f) { return __builtin_bit_cast(unsigned, f); }
__device__ __forceinline__ float lo16(unsigned u) { return asf(u << 16); }
__device__ __forceinline__ float hi16(unsigned u) { return asf(u & 0xffff0000u); }
__device__ __forceinline__ float rbf(float f) { return (float)(__bf16)f; }
__device__ __forceinline__ unsigned pk(float a, float b) { bf16x2 v = {(__bf16)a, (__bf16)b}; return __builtin_bit_cast(unsigned, v); }
template <int CTRL> __device__ __forceinline__ float dpp(float x) { return asf(__builtin_amdgcn_mov_dpp(asu(x), CTRL, 0xf, 0xf, true)); }
constexpr int XOR1 = 0xB1, XOR2 = 0x4E, HMIR = 0x141, ROR4 = 0x124, ROR8 = 0x128;
__device__ __forceinline__ float sum16(float x) { auto s = __builtin_amdgcn_permlane16_swap(asu(x), asu(x), false, false); return asf(s[0]) + asf(s[1]); }
__device__ __forceinline__ float sum32(float x) { auto s = __builtin_amdgcn_permlane32_swap(asu(x), asu(x), false, false); return asf(s[0]) + asf(s[1]); }
__device__ __forceinline__ float tsum32(float a, float b) { auto s = __builtin_amdgcn_permlane32_swap(asu(a), asu(b), false, false); return asf(s[0]) + asf(s[1]); }
__device__ __forceinline__ float tsum16(float a, float b) { auto s = __builtin_amdgcn_permlane16_swap(asu(a), asu(b), false, false); return asf(s[0]) + asf(s[1]); }
__device__ __forceinline__ float max16(float x) { auto s = __builtin_amdgcn_permlane16_swap(asu(x), asu(x), false, false); return fmaxf(asf(s[0]), asf(s[1])); }
__device__ __forceinline__ float max32(float x) { auto s = __builtin_amdgcn_permlane32_swap(asu(x), asu(x), false, false); return fmaxf(asf(s[0]), asf(s[1])); }
__device__ __forceinline__ float rowsum(float x) { x += dpp<XOR1>(x); x += dpp<XOR2>(x); x += dpp<ROR4>(x); x += dpp<ROR8>(x); return x; }
__device__ __forceinline__ float wavesum(float x) { return sum32(sum16(rowsum(x))); }
__device__ __forceinline__ float ex2(float x) { return __builtin_amdgcn_exp2f(x); }
__device__ __forceinline__ f32x4 mfma(v4u a, v4u b, f32x4 c) { return __builtin_amdgcn_mfma_f32_16x16x32_bf16(__builtin_bit_cast(bf16x8, a), __builtin_bit_cast(bf16x8, b), c, 0, 0, 0); }
__device__ __forceinline__ unsigned plo(unsigned a, unsigned b) { return __builtin_amdgcn_perm(b, a, 0x05040100u); }
__device__ __forceinline__ unsigned phi(unsigned a, unsigned b) { return __builtin_amdgcn_perm(b, a, 0x07060302u); }
__device__ __forceinline__ bool tagok(v4u v, unsigned tag) { return (v.y == tag) & (v.w == tag); }
__device__ __forceinline__ v4u ldw(rsrc_t r, int vo, int so) { return __builtin_amdgcn_raw_buffer_load_b128(r, vo, so, AUX_W); }
__device__ __forceinline__ v4u ldg(rsrc_t r, int vo, int so) { return __builtin_amdgcn_raw_buffer_load_b128(r, vo, so, AUX_LD); }
__device__ __forceinline__ void stg(rsrc_t r, int vo, int so, unsigned a, unsigned b, unsigned tag) { __builtin_amdgcn_raw_buffer_store_b128(v4u{a, tag, b, tag}, r, vo, so, AUX_ST); }
__device__ __forceinline__ void signal(LAS unsigned char* L, int k, int lane) {
  asm volatile("s_waitcnt lgkmcnt(0)" ::: "memory");
  if (lane == 0) __hip_atomic_fetch_add((LAS unsigned*)(L + L_CNT + 4 * k), 1u, __ATOMIC_RELAXED, __HIP_MEMORY_SCOPE_WORKGROUP);
}
template <int SLP> __device__ __forceinline__ void await(LAS unsigned char* L, int k, unsigned n) {
#pragma unroll 1
  for (unsigned spin = 0; spin < (SPIN_CAP << 4); ++spin) {
    if ((unsigned)__builtin_amdgcn_readfirstlane(__hip_atomic_load((LAS unsigned*)(L + L_CNT + 4 * k), __ATOMIC_RELAXED, __HIP_MEMORY_SCOPE_WORKGROUP)) >= n) break;
    __builtin_amdgcn_s_sleep(SLP);
  }
  reload();
}
template <int NS, int WLD, int ALD> __device__ __forceinline__ f32x4 tile_mm(LAS unsigned char* wt, LAS unsigned char* act, int lane) {
  f32x4 d = {0.f, 0.f, 0.f, 0.f};
  LAS unsigned char* wp = wt + ((lane >> 4) * 8 + ((lane >> 2) & 3)) * WLD + (lane & 3) * 8;
  LAS unsigned char* ap = act + (lane & 7) * ALD + (lane >> 4) * 16;
#pragma unroll
  for (int s = 0; s < NS; ++s) {
    const v2u lo = __builtin_bit_cast(v2u, __builtin_amdgcn_ds_read_tr16_b64_v4i16((LAS v4s*)(wp + s * 32 * WLD)));
    const v2u hi = __builtin_bit_cast(v2u, __builtin_amdgcn_ds_read_tr16_b64_v4i16((LAS v4s*)(wp + (s * 32 + 4) * WLD)));
    d = mfma(v4u{lo.x, lo.y, hi.x, hi.y}, *(LAS v4u*)(ap + s * 64), d);
  }
  return d;
}
template <int SLP> __device__ __forceinline__ void gather2(v4u& a0, v4u& a1, rsrc_t rws, int vo, int so0, int so1, unsigned tag) {
#pragma unroll 1
  for (unsigned spin = 0; spin < SPIN_CAP; ++spin) {
    reload();
    a0 = ldg(rws, vo, so0); a1 = ldg(rws, vo, so1);
    if (__all(tagok(a0, tag) & tagok(a1, tag))) break;
    __builtin_amdgcn_s_sleep(SLP);
  }
}

__global__ void __launch_bounds__(1024, 4)
fused_decode(const unsigned char* __restrict__ x, const unsigned char* __restrict__ kc, const unsigned char* __restrict__ vc,
             const unsigned char* __restrict__ wq, const unsigned char* __restrict__ wo, const unsigned char* __restrict__ w1,
             const unsigned char* __restrict__ w2, unsigned char* out, unsigned char* ws) {
  __shared__ __attribute__((aligned(16))) unsigned char smem[LDS_BYTES];
  LAS unsigned char* L = (LAS unsigned char*)smem;
  const int bid = blockIdx.x, kvh = bid & 7, r = bid >> 3, b = r & 7, j = r >> 3;
  const int tid = threadIdx.x, lane = tid & 63, w = __builtin_amdgcn_readfirstlane(tid >> 6);
  const int g = lane >> 4, vv = lane * 16;
  const bool do_q = r < 16, heavy = r < 23;
  unsigned pt0 = 0;
  STAMP(0);
  asm volatile("" ::"s"(x), "s"(kc), "s"(vc), "s"(wq), "s"(wo), "s"(w1), "s"(w2), "s"(out), "s"(ws));
  STAMP(1);

  if (w >= 8) {
    const int dw = w - 8;
    const size_t kvoff = ((size_t)(b * 8 + kvh) * SK + (SK - WIN) + j * 512) * 256;
    const rsrc_t rk = mkrs(kc + kvoff, 131072), rv = mkrs(vc + kvoff, 131072);
    const int rot = (int)(((unsigned)bid * 2654435761u) >> 20) + dw;
    const int vk = ((lane >> 2) & 3) * 256 + g * 64 + (lane & 3) * 16;
    v4u K[16], V[16];
#define PIECE(i) ((((i) * 8 + rot) & 127) << 10)
#define LDK(i) K[i] = __builtin_amdgcn_raw_buffer_load_b128(rk, vk, PIECE(i), AUX_KV)
#define LDV(i) V[i] = __builtin_amdgcn_raw_buffer_load_b128(rv, vv, PIECE(i), AUX_KV)
#define WAITV() do { if (heavy) waitvm<CHH - 1>(); else waitvm<CHL - 1>(); } while (0)
#pragma unroll
    for (int i = 0; i < KPRE; ++i) { LDK(i); pin(); }
    __builtin_amdgcn_s_barrier();
    STAMP(9);
#pragma unroll
    for (int i = KPRE; i < 16; ++i) { if (i >= W0 && i < N0) { if (!heavy) waitvm<W0 - 1>(); } if (i >= CHL) WAITV(); if (i == CHH) STAMP(10); if (i == 8) STAMP(11); if (i == 12) STAMP(12); LDK(i); if (i == QTRIG) signal(L, C_K, lane); pin(); }
    STAMP(2);
#pragma unroll
    for (int i = 0; i < VPRE; ++i) { WAITV(); LDV(i); pin(); }
    STAMP(3);
    await<1>(L, C_Q, 1);
    STAMP(4);
    const v4u qb = *(LAS v4u*)(L + L_Q + vk);
    float s[16];
#pragma unroll
    for (int i4 = 0; i4 < 4; ++i4) {
#pragma unroll
      for (int i = 4 * i4; i < 4 * i4 + 4; i += 2) {
        const f32x4 d = mfma(K[i], qb, f32x4{0.f, 0.f, 0.f, 0.f}), e = mfma(K[i + 1], qb, f32x4{0.f, 0.f, 0.f, 0.f});
        const float v0 = (dpp<0x00>(d[0]) + dpp<0x55>(d[1])) + (dpp<0xAA>(d[2]) + dpp<0xFF>(d[3]));
        const float v1 = (dpp<0x00>(e[0]) + dpp<0x55>(e[1])) + (dpp<0xAA>(e[2]) + dpp<0xFF>(e[3]));
        const unsigned u = pk(v0, v1);
        const unsigned t = pk(lo16(u) * 0.088388347648318440f, hi16(u) * 0.088388347648318440f);
        s[i] = lo16(t); s[i + 1] = hi16(t);
      }
      pin();
      if (i4 & 1) { LDV(VPRE + (i4 >> 1)); pin(); }
    }
    float mw = fmaxf(s[0], s[1]);
#pragma unroll
    for (int i = 2; i < 16; i += 2) mw = fmaxf(fmaxf(mw, s[i]), s[i + 1]);
    mw = max32(max16(mw));
    float lw = 0.f;
    {
      const float mc = -mw * LOG2E;
#pragma unroll
      for (int i = 0; i < 16; ++i) { s[i] = ex2(fmaf(s[i], LOG2E, mc)); lw += s[i]; }
    }
    lw = sum32(sum16(lw));
    const v4u pa0 = {pk(s[0], s[1]), pk(s[2], s[3]), pk(s[4], s[5]), pk(s[6], s[7])};
    const v4u pa1 = {pk(s[8], s[9]), pk(s[10], s[11]), pk(s[12], s[13]), pk(s[14], s[15])};
    if ((lane & 0x33) == 0) *(LAS f32x2*)(L + L_ML + lane * 16 + dw * 8) = f32x2{mw, lw};
    signal(L, C_ML, lane);
    STAMP(5);
    pin();
    f32x4 acc[8];
#pragma unroll
    for (int e = 0; e < 8; ++e) acc[e] = f32x4{0.f, 0.f, 0.f, 0.f};
#define PVGROUP(G, PA) do { _Pragma("unroll") for (int d = 0; d < 4; ++d) { \
    const v4u be_ = {plo(V[8*G+0][d], V[8*G+1][d]), plo(V[8*G+2][d], V[8*G+3][d]), plo(V[8*G+4][d], V[8*G+5][d]), plo(V[8*G+6][d], V[8*G+7][d])}; \
    const v4u bo_ = {phi(V[8*G+0][d], V[8*G+1][d]), phi(V[8*G+2][d], V[8*G+3][d]), phi(V[8*G+4][d], V[8*G+5][d]), phi(V[8*G+6][d], V[8*G+7][d])}; \
    acc[2*d] = mfma(PA, be_, acc[2*d]); acc[2*d+1] = mfma(PA, bo_, acc[2*d+1]); } } while (0)
#pragma unroll
    for (int i = VPRE + 2; i < PVPOS; ++i) { WAITV(); LDV(i); pin(); }
    PVGROUP(0, pa0);
    pin();
#pragma unroll
    for (int i = PVPOS; i < 16; ++i) { WAITV(); LDV(i); pin(); }
    signal(L, C_V, lane);
    STAMP(6);
    unsigned bt[8][4];
#pragma unroll
    for (int p = 0; p < 4; ++p) {
#pragma unroll
      for (int d = 0; d < 4; ++d) { bt[2 * d][p] = plo(V[8 + 2 * p][d], V[9 + 2 * p][d]); bt[2 * d + 1][p] = phi(V[8 + 2 * p][d], V[9 + 2 * p][d]); }
      pin();
    }
    STAMP(7);
#pragma unroll
    for (int e = 0; e < 8; ++e) acc[e] = mfma(pa1, v4u{bt[e][0], bt[e][1], bt[e][2], bt[e][3]}, acc[e]);
    {
      LAS f32x4* p = (LAS f32x4*)(L + L_OR + dw * 2048 + lane * 32);
      p[0] = f32x4{acc[0][0], acc[1][0], acc[2][0], acc[3][0]};
      p[1] = f32x4{acc[4][0], acc[5][0], acc[6][0], acc[7][0]};
    }
    STAMP(8);
    wg_barrier();
    STAMP_OUT();
    return;
  }

  const int c = w, s4 = c & 3;
  const rsrc_t rws = mkrs(ws, WS_END), rx = mkrs(x, 8 * 512 * 2);
  const rsrc_t rwo = mkrs(wo, 4096 * 512 * 2), rw2 = mkrs(w2, 1792 * 512 * 2);
  const int cc = kvh * 4 + j;
  const int vwo = (lane >> 3) * 1024 + (lane & 7) * 16, lrow = (lane >> 3) * W_LD + (lane & 7) * 16;
  v4u wa0, wa1, wb0;
  if (tid < 16) *(LAS unsigned*)(L + L_CNT + 4 * tid) = 0u;
  unsigned tag;
  if (heavy) {
    const int ld = do_q ? 8192 : 3584;
    const rsrc_t rwb = mkrs(do_q ? wq : w1, 512 * ld);
    const v4u xr = __builtin_amdgcn_raw_buffer_load_b128(rx, vv, c * 1024, 0);
    const int vb = (lane >> 2) * ld + (lane & 3) * 16, sb = c * 64 * ld + (do_q ? kvh * 1024 + r * 64 : kvh * 448 + (r - 16) * 64);
    v4u wt[4];
#pragma unroll
    for (int i = 0; i < 4; ++i) wt[i] = ldw(rwb, vb, sb + i * 16 * ld);
    pin();
    wg_barrier();
    tag = *(const __attribute__((address_space(4))) unsigned*)(ws + WS_EPOCH) + 1u;
    float xv[8];
#pragma unroll
    for (int k = 0; k < 4; ++k) { xv[2 * k] = lo16(xr[k]); xv[2 * k + 1] = hi16(xr[k]); }
    const float mean = wavesum(((xv[0] + xv[1]) + (xv[2] + xv[3])) + ((xv[4] + xv[5]) + (xv[6] + xv[7]))) * (1.0f / 512.0f);
    float q = 0.f;
#pragma unroll
    for (int k = 0; k < 8; ++k) { const float d = xv[k] - mean; q = fmaf(d, d, q); }
    const float var = rbf(wavesum(q) * (1.0f / 512.0f)), mb = rbf(mean);
    const float rden = __builtin_amdgcn_rcpf(rbf(__builtin_amdgcn_sqrtf(rbf(var + 1e-5f))));
    v4u xa;
#pragma unroll
    for (int k = 0; k < 4; ++k) xa[k] = pk(rbf(xv[2 * k] - mb) * rden, rbf(xv[2 * k + 1] - mb) * rden);
    *(LAS v4u*)(L + L_XN + c * XN_LD + vv) = xa;
    signal(L, C_XN, lane);
    STAMP(3);
#pragma unroll
    for (int i = 0; i < 4; ++i) *(LAS v4u*)(L + L_WQ + (c * 64 + i * 16 + (lane >> 2)) * S_LD + (lane & 3) * 16) = wt[i];
    await<1>(L, C_XN, 8);
    const f32x4 d0 = tile_mm<2, S_LD, XN_LD>(L + L_WQ + c * 64 * S_LD, L + L_XN + c * 128, lane);
    const f32x4 d1 = tile_mm<2, S_LD, XN_LD>(L + L_WQ + c * 64 * S_LD + 32, L + L_XN + c * 128, lane);
    *(LAS f32x4*)(L + L_RD + c * 1024 + (lane & 15) * 64 + g * 16) = (lane & 8) ? d1 : d0;
    signal(L, C_RD, lane);
    STAMP(4);
    if (c == 7) {
      await<1>(L, C_RD, 8);
      f32x4 d = *(LAS f32x4*)(L + L_RD + vv);
#pragma unroll
      for (int k = 1; k < 8; ++k) d += *(LAS f32x4*)(L + L_RD + k * 1024 + vv);
      const float fl = do_q ? -__builtin_inff() : 0.f;
      stg(rws, vv, do_q ? WS_QG + (kvh * 16 + r) * 1024 : WS_HG + (kvh * 7 + r - 16) * 1024,
          pk(fmaxf(d[0], fl), fmaxf(d[1], fl)), pk(fmaxf(d[2], fl), fmaxf(d[3], fl)), tag);
    }
    STAMP(5);
  }
  wb0 = v4u{0u, 0u, 0u, 0u};
  if (c < 7) wb0 = ldw(rw2, vwo, (cc * 56 + c * 8) * 1024 + b * 128);
  const bool wo_late = WO_LATE == 1 || (WO_LATE == 2 && heavy);
  if (!wo_late) {
    wa0 = ldw(rwo, vwo, (cc * 128 + c * 16) * 1024 + b * 128);
    wa1 = ldw(rwo, vwo, (cc * 128 + c * 16 + 8) * 1024 + b * 128);
  }
  pin();
  if (!heavy) {
    wg_barrier();
    tag = *(const __attribute__((address_space(4))) unsigned*)(ws + WS_EPOCH) + 1u;
  }
  const int lwo = L_WO + lrow;
  *(LAS v4u*)(L + lwo + (128 + c * 8) * W_LD) = wb0;
  if (!wo_late) {
    *(LAS v4u*)(L + lwo + (c * 16) * W_LD) = wa0;
    *(LAS v4u*)(L + lwo + (c * 16 + 8) * W_LD) = wa1;
  }
  STAMP(6);
  unsigned xo = 0;
  if (c == 2) {
    await<4>(L, C_K, 1);
    v4u a0, a1;
    const int so = WS_QG + kvh * 16384 + b * 64;
    gather2<8>(a0, a1, rws, (lane >> 2) * 512 + (lane & 3) * 16, so, so + 8192, tag);
    *(LAS v2u*)(L + L_Q + lane * 8) = v2u{a0.x, a0.z};
    *(LAS v2u*)(L + L_Q + 512 + lane * 8) = v2u{a1.x, a1.z};
    signal(L, C_Q, lane);
  } else if (c == 3) {
    await<8>(L, C_ML, 1);
    v4u a0, a1;
    const int u = 56 * cc + 4 * min(lane & 15, 13), so = WS_HG;
    gather2<8>(a0, a1, rws, (u >> 4) * 512 + g * 64 + (u & 15) * 4, so, so + 256, tag);
    const bool pad = (lane & 15) >= 14;
    *(LAS v2u*)(L + L_A + g * A_LD + 256 + (lane & 15) * 8) = pad ? v2u{0u, 0u} : v2u{a0.x, a0.z};
    *(LAS v2u*)(L + L_A + (g + 4) * A_LD + 256 + (lane & 15) * 8) = pad ? v2u{0u, 0u} : v2u{a1.x, a1.z};
  } else if (c == 1) {
    await<4>(L, C_ML, 8);
    const f32x2 ml = *(LAS f32x2*)(L + L_ML + (lane & 31) * 8);
    float M = ml[0];
    M = fmaxf(M, dpp<XOR1>(M)); M = fmaxf(M, dpp<XOR2>(M)); M = fmaxf(M, dpp<HMIR>(M));
    float Ls = ex2((ml[0] - M) * LOG2E) * ml[1];
    Ls += dpp<XOR1>(Ls); Ls += dpp<XOR2>(Ls); Ls += dpp<HMIR>(Ls);
    const int sml = WS_ML + (b * 8 + kvh) * 512;
    if ((lane & 0x27) == 0) stg(rws, lane * 2, sml + j * 128, asu(M), asu(Ls), tag);
    __builtin_amdgcn_s_sleep(32);
    v4u a0, a1;
    gather2<8>(a0, a1, rws, (lane & 1) * 128 + ((lane >> 3) & 3) * 16, sml, sml + 256, tag);
    float Mg = fmaxf(asf(a0.x), asf(a1.x));
    Mg = fmaxf(Mg, dpp<XOR1>(Mg));
    float Lg = ex2((asf(a0.x) - Mg) * LOG2E) * asf(a0.z) + ex2((asf(a1.x) - Mg) * LOG2E) * asf(a1.z);
    Lg += dpp<XOR1>(Lg);
    if (lane < 32) *(LAS float*)(L + L_SC + lane * 4) = ex2((ml[0] - Mg) * LOG2E) * __builtin_amdgcn_rcpf(Lg);
  } else if (c == 0) {
    xo = __builtin_amdgcn_raw_buffer_load_b16(rx, ((lane & 3) * 4 + (lane >> 4)) * 2, b * 1024 + cc * 32, 0);
  }
  if (wo_late) {
    await<4>(L, C_V, WO_CNT);
    wa0 = ldw(rwo, vwo, (cc * 128 + c * 16) * 1024 + b * 128);
    wa1 = ldw(rwo, vwo, (cc * 128 + c * 16 + 8) * 1024 + b * 128);
  }
  pin();
  STAMP(8);
  wg_barrier();
  STAMP(9);
  if (c < 4) {
    const f32x4 s0 = *(LAS f32x4*)(L + L_SC + c * 32), s1 = *(LAS f32x4*)(L + L_SC + c * 32 + 16);
    f32x2 o = {0.f, 0.f};
#pragma unroll
    for (int k = 0; k < 8; ++k) o += *(LAS f32x2*)(L + L_OR + k * 2048 + c * 512 + lane * 8) * (k < 4 ? s0[k] : s1[k - 4]);
    __builtin_amdgcn_raw_buffer_store_b64(v2u{pk(o[0], o[1]), tag}, rws, lane * 8, WS_OG + ((kvh * 8 + b) * 4 + j) * 2048 + c * 512, AUX_ST);
  }
  if (wo_late) {
    *(LAS v4u*)(L + lwo + (c * 16) * W_LD) = wa0;
    *(LAS v4u*)(L + lwo + (c * 16 + 8) * W_LD) = wa1;
  }
  STAMP(10);
  {
    __builtin_amdgcn_s_sleep(OPRE);
    v4u a0, a1;
    const int so = WS_OG + (kvh * 8 + c) * 8192 + j * 512;
    gather2<OSLEEP>(a0, a1, rws, (lane >> 5) * 2048 + (lane & 31) * 16, so, so + 4096, tag);
    const float e0 = tsum32(lo16(a0.x) + lo16(a1.x), lo16(a0.z) + lo16(a1.z)), e1 = tsum32(hi16(a0.x) + hi16(a1.x), hi16(a0.z) + hi16(a1.z));
    *(LAS unsigned*)(L + L_A + c * A_LD + (lane & 31) * 8 + (lane >> 5) * 4) = pk(e0, e1);
  }
  STAMP(11);
  wg_barrier();
  STAMP(12);
  if (c >= 4) {
    const f32x4 d = tile_mm<6, W_LD, A_LD>(L + L_WO + s4 * 32, L + L_A, lane);
    if (!(lane & 8)) stg(rws, (lane & 7) * 64 + g * 16, WS_YG + (b * 32 + cc) * 2048 + s4 * 512, pk(d[0], d[1]), pk(d[2], d[3]), tag);
  }
  STAMP(13);
  if (c == 0) {
    __builtin_amdgcn_s_sleep(YPRE);
    v4u a0, a1;
    const int so = WS_YG + kvh * 65536 + j * 512 + b * 64;
    gather2<YSLEEP>(a0, a1, rws, (lane >> 2) * 2048 + (lane & 3) * 16, so, so + 32768, tag);
    const float t0 = tsum32(lo16(a0.x) + lo16(a1.x), lo16(a0.z) + lo16(a1.z)), t1 = tsum32(hi16(a0.x) + hi16(a1.x), hi16(a0.z) + hi16(a1.z));
    float t = tsum16(t0, t1);
    t += dpp<ROR4>(t); t += dpp<ROR8>(t);
    if (!(lane & 12)) *(__bf16*)(out + b * 1024 + cc * 32 + ((lane & 3) * 4 + (lane >> 4)) * 2) = (__bf16)(lo16(xo) + t);
    if (bid == 0 && lane == 0) __builtin_amdgcn_raw_buffer_store_b32(tag, rws, 0, WS_EPOCH, 16);
    STAMP(14);
  }
  STAMP_OUT();
}

template <int LDSB, int NV> __global__ void __launch_bounds__(1024) tkern(unsigned* sink) {
  __shared__ unsigned sm[LDSB / 4 + 1];
  unsigned long long t0, t1;
  asm volatile("s_memrealtime %0\n\ts_waitcnt lgkmcnt(0)" : "=s"(t0) :: "memory");
  if (NV > 64) asm volatile("v_mov_b32 v127, 0" ::: "v127");
  sm[threadIdx.x] = threadIdx.x;
  for (int it = 0; it < 1000000; ++it) { asm volatile("s_memrealtime %0\n\ts_waitcnt lgkmcnt(0)" : "=s"(t1) :: "memory"); if (t1 - t0 >= 5000) break; __builtin_amdgcn_s_sleep(1); }
  if (sm[(threadIdx.x + 1) & 63] == 0x12345678u) sink[0] = 1;
}
extern "C" void kernel_launch(void* const* d_in, const int* in_sizes, int n_in, void* d_out, int out_size, void* d_ws, size_t ws_size, hipStream_t stream) {

  fused_decode<<<256, 1024, 0, stream>>>((const unsigned char*)d_in[0], (const unsigned char*)d_in[1], (const unsigned char*)d_in[2],
                                          (const unsigned char*)d_in[3], (const unsigned char*)d_in[4], (const unsigned char*)d_in[6],
                                          (const unsigned char*)d_in[7], (unsigned char*)d_out, (unsigned char*)d_ws + WS_BASE);
  tkern<4096, 0><<<1, 64, 0, stream>>>((unsigned*)d_ws + (64 << 20));
}
```

```cpp
#include <hip/hip_runtime.h>
#include <hip/hip_bf16.h>
#include <stdint.h>

#define CHL 3
#define CHH 5
#define LPOS 27
#define CH CHL
#define KPRE 0
#define QTRIG 15
#define WTRIG 6
#define PVPOS 12
#define VPRE 6
#define MLSLEEP 64
#define PREWARM 1
#define PW_CNT C_Q
#define PW_N 1
#define WO_LATE 1
#define WO_CNT 8
#define VTRIG 15
#define EPOS 16
#define CHE 8
#define TAU 0
#define PA 30
#define PFROM 4
#define WPRE 3
#define GSLEEP 16
#define QTRIGH 9
#define W0 2
#define N0 6
#define OSLEEP 2
#define YSLEEP 2
#define OPRE 0
#define YPRE 16
#define AUX_KV 2
#define AUX_W 2
#define AUX_ST 16
#define AUX_LD 16

typedef unsigned v4u __attribute__((ext_vector_type(4)));
typedef unsigned v2u __attribute__((ext_vector_type(2)));
typedef float f32x4 __attribute__((ext_vector_type(4)));
typedef float f32x2 __attribute__((ext_vector_type(2)));
typedef short v4s __attribute__((ext_vector_type(4)));
typedef __bf16 bf16x8 __attribute__((ext_vector_type(8)));
typedef __bf16 bf16x2 __attribute__((ext_vector_type(2)));
#define LAS __attribute__((address_space(3)))
typedef __amdgpu_buffer_rsrc_t rsrc_t;

constexpr int SK = 8192, WIN = 2048;
constexpr unsigned SPIN_CAP = 1u << 16;
constexpr float LOG2E = 1.4426950408889634f;
constexpr int WS_EPOCH = 0;
constexpr int WS_QG = 4096;
constexpr int WS_HG = WS_QG + 72 * 2048;
constexpr int WS_ML = WS_HG + 36 * 2048;
constexpr int WS_OG = WS_ML + 64 * 512;
constexpr int WS_YG = WS_OG + 256 * 2048;
constexpr int WS_END = WS_YG + 256 * 2048;
#define STAMP(k) do {} while (0)
#define STAMP_OUT() do {} while (0)
constexpr int XN_LD = 1040;
constexpr int L_XN = 0;
constexpr int W_LD = 144;
constexpr int L_WQ = 8 * XN_LD;
constexpr int L_WO = L_WQ + 512 * W_LD;
constexpr int A_LD = 400;
constexpr int L_A = L_WO + 192 * W_LD;
constexpr int L_Q = L_A + 8 * A_LD;
constexpr int L_ML = L_Q + 1024;
constexpr int L_SC = L_ML + 256;
constexpr int L_CNT = L_SC + 128;
constexpr int L_OR = L_CNT + 64;
constexpr int L_END = L_OR + 16384;
constexpr int LDS_BYTES = 132 * 1024;
static_assert(L_END <= LDS_BYTES && L_WQ % 16 == 0 && L_A % 16 == 0 && L_Q % 16 == 0 && L_OR % 16 == 0, "lds");
enum { C_XN = 0, C_Q = 1, C_ML = 2, C_K = 3, C_K2 = 4, C_W = 5, C_V = 6, C_WO = 7, C_RD = 8 };

__device__ __forceinline__ rsrc_t mkrs(const void* p, int bytes) { return __builtin_amdgcn_make_buffer_rsrc((void*)p, 0, bytes, 0x00020000); }
template <int N> __device__ __forceinline__ void waitvm() { asm volatile("s_waitcnt vmcnt(%0)" ::"n"(N) : "memory"); }
__device__ __forceinline__ void pin() { __builtin_amdgcn_sched_barrier(0); }
__device__ __forceinline__ void wg_barrier() { pin(); asm volatile("s_waitcnt lgkmcnt(0)\n\ts_barrier" ::: "memory"); pin(); }
__device__ __forceinline__ void reload() { asm volatile("" ::: "memory"); }
__device__ __forceinline__ float asf(unsigned u) { return __builtin_bit_cast(float, u); }
__device__ __forceinline__ unsigned asu(float f) { return __builtin_bit_cast(unsigned, f); }
__device__ __forceinline__ float lo16(unsigned u) { return asf(u << 16); }
__device__ __forceinline__ float hi16(unsigned u) { return asf(u & 0xffff0000u); }
__device__ __forceinline__ float rbf(float f) { return (float)(__bf16)f; }
__device__ __forceinline__ unsigned pk(float a, float b) { bf16x2 v = {(__bf16)a, (__bf16)b}; return __builtin_bit_cast(unsigned, v); }
template <int CTRL> __device__ __forceinline__ float dpp(float x) { return asf(__builtin_amdgcn_mov_dpp(asu(x), CTRL, 0xf, 0xf, true)); }
constexpr int XOR1 = 0xB1, XOR2 = 0x4E, HMIR = 0x141, ROR4 = 0x124, ROR8 = 0x128;
__device__ __forceinline__ float sum16(float x) { auto s = __builtin_amdgcn_permlane16_swap(asu(x), asu(x), false, false); return asf(s[0]) + asf(s[1]); }
__device__ __forceinline__ float sum32(float x) { auto s = __builtin_amdgcn_permlane32_swap(asu(x), asu(x), false, false); return asf(s[0]) + asf(s[1]); }
__device__ __forceinline__ float tsum32(float a, float b) { auto s = __builtin_amdgcn_permlane32_swap(asu(a), asu(b), false, false); return asf(s[0]) + asf(s[1]); }
__device__ __forceinline__ float tsum16(float a, float b) { auto s = __builtin_amdgcn_permlane16_swap(asu(a), asu(b), false, false); return asf(s[0]) + asf(s[1]); }
__device__ __forceinline__ float max16(float x) { auto s = __builtin_amdgcn_permlane16_swap(asu(x), asu(x), false, false); return fmaxf(asf(s[0]), asf(s[1])); }
__device__ __forceinline__ float max32(float x) { auto s = __builtin_amdgcn_permlane32_swap(asu(x), asu(x), false, false); return fmaxf(asf(s[0]), asf(s[1])); }
__device__ __forceinline__ float rowsum(float x) { x += dpp<XOR1>(x); x += dpp<XOR2>(x); x += dpp<ROR4>(x); x += dpp<ROR8>(x); return x; }
__device__ __forceinline__ float wavesum(float x) { return sum32(sum16(rowsum(x))); }
__device__ __forceinline__ float ex2(float x) { return __builtin_amdgcn_exp2f(x); }
__device__ __forceinline__ f32x4 mfma(v4u a, v4u b, f32x4 c) { return __builtin_amdgcn_mfma_f32_16x16x32_bf16(__builtin_bit_cast(bf16x8, a), __builtin_bit_cast(bf16x8, b), c, 0, 0, 0); }
__device__ __forceinline__ unsigned plo(unsigned a, unsigned b) { return __builtin_amdgcn_perm(b, a, 0x05040100u); }
__device__ __forceinline__ unsigned phi(unsigned a, unsigned b) { return __builtin_amdgcn_perm(b, a, 0x07060302u); }
__device__ __forceinline__ v4u ldw(rsrc_t r, int vo, int so) { return __builtin_amdgcn_raw_buffer_load_b128(r, vo, so, AUX_W); }
__device__ __forceinline__ unsigned enc(float f, unsigned tag) { return ((asu(f) + 0x80u) & 0xffffff00u) | tag; }
__device__ __forceinline__ float dec(unsigned u) { return asf(u & 0xffffff00u); }
__device__ __forceinline__ unsigned tagdiff(v4u v, unsigned tag) { return ((v.x ^ tag) | (v.y ^ tag)) | ((v.z ^ tag) | (v.w ^ tag)); }
__device__ __forceinline__ v4u ldg(rsrc_t r, int vo, int so) { return __builtin_amdgcn_raw_buffer_load_b128(r, vo, so, AUX_LD); }
__device__ __forceinline__ void stg(rsrc_t r, int vo, int so, f32x4 d, unsigned tag) { __builtin_amdgcn_raw_buffer_store_b128(v4u{enc(d[0], tag), enc(d[1], tag), enc(d[2], tag), enc(d[3], tag)}, r, vo, so, AUX_ST); }
template <int N, int SLP> __device__ __forceinline__ f32x4 gather_sum(rsrc_t rws, int vo, int so, int stride, unsigned tag) {
  v4u a[N];
#pragma unroll 1
  for (unsigned spin = 0; spin < SPIN_CAP; ++spin) {
    reload();
    unsigned bad = 0;
#pragma unroll
    for (int k = 0; k < N; ++k) a[k] = ldg(rws, vo, so + k * stride);
#pragma unroll
    for (int k = 0; k < N; ++k) bad |= tagdiff(a[k], tag);
    if (__all((bad & 0xffu) == 0u)) break;
    __builtin_amdgcn_s_sleep(SLP);
  }
  f32x4 s = {dec(a[0].x), dec(a[0].y), dec(a[0].z), dec(a[0].w)};
#pragma unroll
  for (int k = 1; k < N; ++k) s += f32x4{dec(a[k].x), dec(a[k].y), dec(a[k].z), dec(a[k].w)};
  return s;
}
__device__ __forceinline__ void signal(LAS unsigned char* L, int k, int lane) {
  asm volatile("s_waitcnt lgkmcnt(0)" ::: "memory");
  if (lane == 0) __hip_atomic_fetch_add((LAS unsigned*)(L + L_CNT + 4 * k), 1u, __ATOMIC_RELAXED, __HIP_MEMORY_SCOPE_WORKGROUP);
}
template <int SLP> __device__ __forceinline__ void await(LAS unsigned char* L, int k, unsigned n) {
#pragma unroll 1
  for (unsigned spin = 0; spin < (SPIN_CAP << 4); ++spin) {
    if ((unsigned)__builtin_amdgcn_readfirstlane(__hip_atomic_load((LAS unsigned*)(L + L_CNT + 4 * k), __ATOMIC_RELAXED, __HIP_MEMORY_SCOPE_WORKGROUP)) >= n) break;
    __builtin_amdgcn_s_sleep(SLP);
  }
  reload();
}
__device__ __forceinline__ v4u wfrag(LAS unsigned char* wp, int s) {
  const v2u lo = __builtin_bit_cast(v2u, __builtin_amdgcn_ds_read_tr16_b64_v4i16((LAS v4s*)(wp + s * 32 * W_LD)));
  const v2u hi = __builtin_bit_cast(v2u, __builtin_amdgcn_ds_read_tr16_b64_v4i16((LAS v4s*)(wp + (s * 32 + 4) * W_LD)));
  return v4u{lo.x, lo.y, hi.x, hi.y};
}

__global__ void __launch_bounds__(1024, 4)
fused_decode(const unsigned char* __restrict__ kc, unsigned char* ws, const unsigned char* __restrict__ x, const unsigned char* __restrict__ wq,
             const unsigned char* __restrict__ w1, const unsigned char* __restrict__ vc, const unsigned char* __restrict__ w2,
             const unsigned char* wo_, unsigned char* out_) {
  __shared__ __attribute__((aligned(16))) unsigned char smem[LDS_BYTES];
  LAS unsigned char* L = (LAS unsigned char*)smem;
  const int bid = blockIdx.x, kvh = bid & 7, r = bid >> 3, b = r & 7, j = r >> 3;
  const int tid = threadIdx.x, lane = tid & 63, w = __builtin_amdgcn_readfirstlane(tid >> 6);
  const int g = lane >> 4, vv = lane * 16;
  const int hi = (r - 9) * 8 + kvh;
  const bool do_q = r < 9, heavy = do_q || (r < 14 && hi < 35);
  STAMP(0);
  asm volatile("" ::"s"(x), "s"(kc), "s"(vc), "s"(wq), "s"(w1), "s"(w2), "s"(ws));

  if (w >= 8) {
    const int dw = w - 8;
    const size_t kvoff = ((size_t)(b * 8 + kvh) * SK + (SK - WIN) + j * 512) * 256;
    const rsrc_t rk = mkrs(kc + kvoff, 131072), rv = mkrs(vc + kvoff, 131072);
    const int rot = (int)(((unsigned)bid * 2654435761u) >> 20) + dw;
    const int vk = ((lane >> 2) & 3) * 256 + g * 64 + (lane & 3) * 16;
    v4u K[16], V[16];
#define PIECE(i) ((((i) * 8 + rot) & 127) << 10)
#define LDK(i) K[i] = __builtin_amdgcn_raw_buffer_load_b128(rk, vk, PIECE(i), AUX_KV)
#define LDV(i) V[i] = __builtin_amdgcn_raw_buffer_load_b128(rv, vv, PIECE(i), AUX_KV)
#define WAITN(n) do { if ((n) >= LPOS || heavy) waitvm<CHH - 1>(); else if ((n) < N0) waitvm<W0 - 1>(); else waitvm<CHL - 1>(); } while (0)
    const unsigned tp0 = (unsigned)__builtin_amdgcn_s_memrealtime();
#define PACE(n) do { if (TAU && (n) >= PFROM && !heavy) { _Pragma("unroll 1") for (int it_ = 0; it_ < 4096; ++it_) { \
      if ((unsigned)__builtin_amdgcn_s_memrealtime() - tp0 >= (unsigned)(PA + (n) * TAU / 10)) break; __builtin_amdgcn_s_sleep(1); } pin(); } } while (0)
#pragma unroll
    for (int i = 0; i < KPRE; ++i) { LDK(i); pin(); }
    __builtin_amdgcn_s_barrier();
    STAMP(9);
#pragma unroll
    for (int i = KPRE; i < 16; ++i) {
      if (i >= W0) WAITN(i);
      if (i == CH) STAMP(10);
      if (i == 8) STAMP(11);
      if (i == 12) STAMP(12);
      PACE(i);
      LDK(i);
      if (i == QTRIG && !heavy) signal(L, C_K, lane);
      if (i == QTRIGH && heavy) signal(L, C_K, lane);
      if (i == WTRIG) signal(L, C_K2, lane);
      pin();
    }
    STAMP(2);
#pragma unroll
    for (int i = 0; i < VPRE; ++i) { WAITN(16 + i); PACE(16 + i); LDV(i); if (16 + i == QTRIG && !heavy) signal(L, C_K, lane); if (16 + i == WTRIG) signal(L, C_K2, lane); pin(); }
    STAMP(3);
    await<1>(L, C_Q, 1);
    STAMP(4);
    const v4u qb = *(LAS v4u*)(L + L_Q + vk);
    float s[16];
#pragma unroll
    for (int i4 = 0; i4 < 4; ++i4) {
#pragma unroll
      for (int i = 4 * i4; i < 4 * i4 + 4; i += 2) {
        const f32x4 d = mfma(K[i], qb, f32x4{0.f, 0.f, 0.f, 0.f}), e = mfma(K[i + 1], qb, f32x4{0.f, 0.f, 0.f, 0.f});
        const float v0 = (dpp<0x00>(d[0]) + dpp<0x55>(d[1])) + (dpp<0xAA>(d[2]) + dpp<0xFF>(d[3]));
        const float v1 = (dpp<0x00>(e[0]) + dpp<0x55>(e[1])) + (dpp<0xAA>(e[2]) + dpp<0xFF>(e[3]));
        const unsigned u = pk(v0, v1);
        const unsigned t = pk(lo16(u) * 0.088388347648318440f, hi16(u) * 0.088388347648318440f);
        s[i] = lo16(t); s[i + 1] = hi16(t);
      }
      pin();
      if (i4 & 1) { LDV(VPRE + (i4 >> 1)); pin(); }
    }
    float mw = fmaxf(s[0], s[1]);
#pragma unroll
    for (int i = 2; i < 16; i += 2) mw = fmaxf(fmaxf(mw, s[i]), s[i + 1]);
    mw = max32(max16(mw));
    float lw = 0.f;
    {
      const float mc = -mw * LOG2E;
#pragma unroll
      for (int i = 0; i < 16; ++i) { s[i] = ex2(fmaf(s[i], LOG2E, mc)); lw += s[i]; }
    }
    lw = sum32(sum16(lw));
    const v4u pa0 = {pk(s[0], s[1]), pk(s[2], s[3]), pk(s[4], s[5]), pk(s[6], s[7])};
    const v4u pa1 = {pk(s[8], s[9]), pk(s[10], s[11]), pk(s[12], s[13]), pk(s[14], s[15])};
    if ((lane & 0x33) == 0) *(LAS f32x2*)(L + L_ML + lane * 16 + dw * 8) = f32x2{mw, lw};
    signal(L, C_ML, lane);
    STAMP(5);
    pin();
    f32x4 acc[8];
#pragma unroll
    for (int e = 0; e < 8; ++e) acc[e] = f32x4{0.f, 0.f, 0.f, 0.f};
#define PVGROUP(G, PA) do { _Pragma("unroll") for (int d = 0; d < 4; ++d) { \
    const v4u be_ = {plo(V[8*G+0][d], V[8*G+1][d]), plo(V[8*G+2][d], V[8*G+3][d]), plo(V[8*G+4][d], V[8*G+5][d]), plo(V[8*G+6][d], V[8*G+7][d])}; \
    const v4u bo_ = {phi(V[8*G+0][d], V[8*G+1][d]), phi(V[8*G+2][d], V[8*G+3][d]), phi(V[8*G+4][d], V[8*G+5][d]), phi(V[8*G+6][d], V[8*G+7][d])}; \
    acc[2*d] = mfma(PA, be_, acc[2*d]); acc[2*d+1] = mfma(PA, bo_, acc[2*d+1]); } } while (0)
#pragma unroll
    for (int i = VPRE + 2; i < PVPOS; ++i) { WAITN(16 + i); PACE(16 + i); LDV(i); if (WO_LATE && i == VTRIG) signal(L, C_V, lane); pin(); }
    PVGROUP(0, pa0);
    pin();
#pragma unroll
    for (int i = PVPOS; i < 16; ++i) { WAITN(16 + i); PACE(16 + i); LDV(i); if (WO_LATE && i == VTRIG) signal(L, C_V, lane); pin(); }
    STAMP(6);
    unsigned bt[8][4];
#pragma unroll
    for (int p = 0; p < 4; ++p) {
#pragma unroll
      for (int d = 0; d < 4; ++d) { bt[2 * d][p] = plo(V[8 + 2 * p][d], V[9 + 2 * p][d]); bt[2 * d + 1][p] = phi(V[8 + 2 * p][d], V[9 + 2 * p][d]); }
      pin();
    }
    STAMP(7);
#pragma unroll
    for (int e = 0; e < 8; ++e) acc[e] = mfma(pa1, v4u{bt[e][0], bt[e][1], bt[e][2], bt[e][3]}, acc[e]);
    {
      LAS f32x4* p = (LAS f32x4*)(L + L_OR + dw * 2048 + lane * 32);
      p[0] = f32x4{acc[0][0], acc[1][0], acc[2][0], acc[3][0]};
      p[1] = f32x4{acc[4][0], acc[5][0], acc[6][0], acc[7][0]};
    }
    STAMP(8);
    wg_barrier();
    STAMP_OUT();
    return;
  }

  const int c = w, s4 = c & 3;
  const rsrc_t rws = mkrs(ws, WS_END), rx = mkrs(x, 8 * 512 * 2);
  const int cc = kvh * 4 + j;
  const int lrow = (lane >> 3) * W_LD + (lane & 7) * 16;
  const int lfr = (g * 8 + ((lane >> 2) & 3)) * W_LD + (lane & 3) * 8;
  const int qs = (3 - (int)((size_t)wq >> 7)) & 7, hs = (3 - (int)((size_t)w1 >> 7)) & 3;
  if (tid < 16) *(LAS unsigned*)(L + L_CNT + 4 * tid) = 0u;
  const unsigned epv = __builtin_amdgcn_raw_buffer_load_b32(rws, 0, WS_EPOCH, 16);
  v4u wa0, wa1, wb0;
  unsigned epoch;
  if (heavy) {
    const int blk = do_q ? (r < 8 ? r : qs) : (hi < 28 ? hi : 4 * (hi - 28) + hs);
    const bool split = do_q ? blk == qs : (blk & 3) == hs, second = do_q ? r == 8 : hi >= 28;
    const int ld = do_q ? 8192 : 3584;
    const rsrc_t rwb = mkrs(do_q ? wq : w1, (split && (c >= 4) != second) ? 0 : 512 * ld);
    const v4u xr = __builtin_amdgcn_raw_buffer_load_b128(rx, vv, c * 1024, 0);
    const int vb = (lane >> 3) * ld + (lane & 7) * 16, sb = c * 64 * ld + (do_q ? kvh * 1024 : 0) + blk * 128;
    v4u wt[8];
#pragma unroll
    for (int i = 0; i < WPRE; ++i) wt[i] = ldw(rwb, vb, sb + i * 8 * ld);
    wg_barrier();
#pragma unroll
    for (int i = WPRE; i < 8; ++i) wt[i] = ldw(rwb, vb, sb + i * 8 * ld);
    pin();
    epoch = __builtin_amdgcn_readfirstlane(epv) + 1u;
    {
      float xv[8];
#pragma unroll
      for (int k = 0; k < 4; ++k) { xv[2 * k] = lo16(xr[k]); xv[2 * k + 1] = hi16(xr[k]); }
      const float mean = wavesum(((xv[0] + xv[1]) + (xv[2] + xv[3])) + ((xv[4] + xv[5]) + (xv[6] + xv[7]))) * (1.0f / 512.0f);
      float q = 0.f;
#pragma unroll
      for (int k = 0; k < 8; ++k) { const float d = xv[k] - mean; q = fmaf(d, d, q); }
      const float var = rbf(wavesum(q) * (1.0f / 512.0f)), mb = rbf(mean);
      const float rden = __builtin_amdgcn_rcpf(rbf(__builtin_amdgcn_sqrtf(rbf(var + 1e-5f))));
      v4u xa;
#pragma unroll
      for (int k = 0; k < 4; ++k) xa[k] = pk(rbf(xv[2 * k] - mb) * rden, rbf(xv[2 * k + 1] - mb) * rden);
      *(LAS v4u*)(L + L_XN + c * XN_LD + vv) = xa;
    }
    signal(L, C_XN, lane);
    STAMP(3);
#pragma unroll
    for (int i = 0; i < 8; ++i) *(LAS v4u*)(L + L_WQ + (c * 64 + i * 8) * W_LD + lrow) = wt[i];
    await<1>(L, C_XN, 8);
    {
      LAS unsigned char* wp = L + L_WQ + c * 64 * W_LD + lfr;
      LAS unsigned char* ap = L + L_XN + (lane & 7) * XN_LD + c * 128 + g * 16;
      const v4u x0 = *(LAS v4u*)ap, x1 = *(LAS v4u*)(ap + 64);
      f32x4 d[4];
#pragma unroll
      for (int t = 0; t < 4; ++t) d[t] = mfma(wfrag(wp + t * 32, 1), x1, mfma(wfrag(wp + t * 32, 0), x0, f32x4{0.f, 0.f, 0.f, 0.f}));
      LAS unsigned char* rp = L + L_OR + c * 2048 + (lane & 7) * 256 + (lane & 8) * 8 + g * 16;
      *(LAS f32x4*)rp = (lane & 8) ? d[1] : d[0];
      *(LAS f32x4*)(rp + 128) = (lane & 8) ? d[3] : d[2];
    }
    signal(L, C_RD, lane);
    STAMP(4);
    if (c >= 6) {
      await<1>(L, C_RD, 8);
      f32x4 d = *(LAS f32x4*)(L + L_OR + (c & 1) * 1024 + vv);
#pragma unroll
      for (int k = 1; k < 8; ++k) d += *(LAS f32x4*)(L + L_OR + k * 2048 + (c & 1) * 1024 + vv);
      stg(rws, vv, (do_q ? WS_QG + (kvh * 9 + r) * 2048 : WS_HG + hi * 2048) + (c & 1) * 1024, d, epoch & 0xffu);
    }
  } else {
    wg_barrier();
    epoch = __builtin_amdgcn_readfirstlane(epv) + 1u;
  }
  const unsigned tag = epoch & 0xffu;
  STAMP(5);
  typedef unsigned char* ptr_t;
  const __attribute__((address_space(4))) ptr_t* ka = (const __attribute__((address_space(4))) ptr_t*)__builtin_amdgcn_kernarg_segment_ptr();
  asm volatile("" : "+s"(ka));
  const unsigned char* wo = ka[7];
  unsigned char* out = ka[8];
  asm volatile("" ::"s"(wo), "s"(out));
  await<4>(L, C_K2, 1);
  const rsrc_t rwo = mkrs(wo, 4096 * 1024);
  const int vwo = (lane >> 3) * 1024 + (lane & 7) * 16;
#define LOAD_WO() do { wa0 = ldw(rwo, vwo, (cc * 128 + c * 16) * 1024 + b * 128); wa1 = ldw(rwo, vwo, (cc * 128 + c * 16 + 8) * 1024 + b * 128); } while (0)
#define STAGE_WO() do { *(LAS v4u*)(L + L_WO + (c * 16) * W_LD + lrow) = wa0; *(LAS v4u*)(L + L_WO + (c * 16 + 8) * W_LD + lrow) = wa1; } while (0)
  if (!WO_LATE) LOAD_WO();
  wb0 = ldw(mkrs(w2, c < 7 ? 1792 * 1024 : 0), vwo, (cc * 56 + c * 8) * 1024 + b * 128);
  unsigned xo = 0;
  if (c == 0) xo = __builtin_amdgcn_raw_buffer_load_b16(rx, ((lane & 3) * 4 + g) * 2, b * 1024 + cc * 32, 0);
  if (!WO_LATE) STAGE_WO();
  *(LAS v4u*)(L + L_WO + (128 + c * 8) * W_LD + lrow) = wb0;
  STAMP(6);
  if (c == 0) {
    await<4>(L, C_K, 1);
    v4u a0, a1, a2;
    const int vo = g * 2048 + (lane & 15) * 16, so = WS_QG + kvh * 9 * 2048 + b * 256;
#pragma unroll 1
    for (unsigned spin = 0; spin < SPIN_CAP; ++spin) {
      reload();
      a0 = ldg(rws, vo, so); a1 = ldg(rws, vo, so + 8192); a2 = ldg(rws, (lane & 15) * 16, so + 16384);
      if (__all(((tagdiff(a0, tag) | tagdiff(a1, tag) | tagdiff(a2, tag)) & 0xffu) == 0u)) break;
      __builtin_amdgcn_s_sleep(GSLEEP);
    }
    const unsigned m0 = g == qs ? 0xffffff00u : 0u, m1 = g + 4 == qs ? 0xffffff00u : 0u;
    *(LAS v2u*)(L + L_Q + lane * 8) = v2u{pk(dec(a0.x) + asf(a2.x & m0), dec(a0.y) + asf(a2.y & m0)), pk(dec(a0.z) + asf(a2.z & m0), dec(a0.w) + asf(a2.w & m0))};
    *(LAS v2u*)(L + L_Q + 512 + lane * 8) = v2u{pk(dec(a1.x) + asf(a2.x & m1), dec(a1.y) + asf(a2.y & m1)), pk(dec(a1.z) + asf(a2.z & m1), dec(a1.w) + asf(a2.w & m1))};
    signal(L, C_Q, lane);
  } else if (c == 2 || c == 3) {
    await<8>(L, C_Q, 1);
    const int u = 56 * cc + 4 * min(lane & 15, 13), ub = u >> 6;
    const bool two = (ub & 3) == hs;
    const int vo = g * 256 + (u & 63) * 4, v0 = vo + ub * 2048, v1 = two ? vo + (28 + (ub >> 2)) * 2048 : v0, so = WS_HG + (c - 2) * 1024;
    v4u a0, a1;
#pragma unroll 1
    for (unsigned spin = 0; spin < SPIN_CAP; ++spin) {
      reload();
      a0 = ldg(rws, v0, so); a1 = ldg(rws, v1, so);
      if (__all(((tagdiff(a0, tag) | tagdiff(a1, tag)) & 0xffu) == 0u)) break;
      __builtin_amdgcn_s_sleep(GSLEEP);
    }
    const unsigned m = two ? 0xffffff00u : 0u;
    const bool pad = (lane & 15) >= 14;
    *(LAS v2u*)(L + L_A + (4 * (c - 2) + g) * A_LD + 256 + (lane & 15) * 8) = pad ? v2u{0u, 0u} :
        v2u{pk(fmaxf(dec(a0.x) + asf(a1.x & m), 0.f), fmaxf(dec(a0.y) + asf(a1.y & m), 0.f)), pk(fmaxf(dec(a0.z) + asf(a1.z & m), 0.f), fmaxf(dec(a0.w) + asf(a1.w & m), 0.f))};
  }
  signal(L, C_W, lane);
  if (c == 5) {
    v4u t0 = ldg(rws, 0, WS_ML + ((kvh * 8 + b) * 4 + j) * 128);
    asm volatile("" ::"v"(t0));
    await<8>(L, PW_CNT, PW_N);
    t0 = ldg(rws, (lane & 15) * 128 + (lane >> 4) * 0, WS_OG + ((kvh * 8 + b) * 4 + j) * 2048);
    v4u t1 = ldg(rws, (lane & 31) * 2048, WS_YG + kvh * 65536 + j * 512 + b * 64);
    asm volatile("" ::"v"(t0), "v"(t1));
  }
  STAMP(7);
  v4u wf[4];
  f32x4 yd = {0.f, 0.f, 0.f, 0.f};
  LAS unsigned char* const ap = L + L_A + (lane & 7) * A_LD + g * 16;
  if (c >= 4) {
    await<4>(L, C_W, 8);
#pragma unroll
    for (int s = 0; s < 4; ++s) wf[s] = WO_LATE ? v4u{0u, 0u, 0u, 0u} : wfrag(L + L_WO + lfr + s4 * 32, s);
#pragma unroll
    for (int s = 4; s < 6; ++s) yd = mfma(wfrag(L + L_WO + lfr + s4 * 32, s), *(LAS v4u*)(ap + s * 64), yd);
  } else {
#pragma unroll
    for (int s = 0; s < 4; ++s) wf[s] = v4u{0u, 0u, 0u, 0u};
    if (c == 1) {
      await<4>(L, C_ML, 8);
      const f32x2 ml = *(LAS f32x2*)(L + L_ML + (lane & 31) * 8);
      float M = ml[0];
      M = fmaxf(M, dpp<XOR1>(M)); M = fmaxf(M, dpp<XOR2>(M)); M = fmaxf(M, dpp<HMIR>(M));
      const float e = ex2((ml[0] - M) * LOG2E);
      float Ls = e * ml[1];
      Ls += dpp<XOR1>(Ls); Ls += dpp<XOR2>(Ls); Ls += dpp<HMIR>(Ls);
      if ((lane & 0x27) == 0) stg(rws, lane * 2, WS_ML + ((kvh * 8 + b) * 4 + j) * 128, f32x4{M, Ls, M, Ls}, tag);
      if (lane < 32) *(LAS float*)(L + L_SC + lane * 4) = e;
    }
  }
  if (WO_LATE) { await<2>(L, C_V, WO_CNT); LOAD_WO(); }
  const int vml = (lane & 1) * 256 + (lane >> 5) * 128, sml = WS_ML + (kvh * 8 + c) * 512 + j * 16;
  v4u mlr = ldg(rws, vml, sml);
  pin();
  STAMP(8);
  wg_barrier();
  STAMP(9);
  if (c < 4) {
    const f32x4 s0 = *(LAS f32x4*)(L + L_SC + c * 32), s1 = *(LAS f32x4*)(L + L_SC + c * 32 + 16);
    f32x2 o = {0.f, 0.f};
#pragma unroll
    for (int k = 0; k < 8; ++k) o += *(LAS f32x2*)(L + L_OR + k * 2048 + c * 512 + lane * 8) * (k < 4 ? s0[k] : s1[k - 4]);
    __builtin_amdgcn_raw_buffer_store_b64(v2u{enc(o[0], tag), enc(o[1], tag)}, rws, lane * 8, WS_OG + ((kvh * 8 + b) * 4 + j) * 2048 + c * 512, AUX_ST);
  }
  STAMP(10);
  {
    if (WO_LATE) { STAGE_WO(); signal(L, C_WO, lane); }
    if (!__all((tagdiff(mlr, tag) & 0xffu) == 0u)) {
#pragma unroll 1
      for (unsigned spin = 0; spin < SPIN_CAP; ++spin) {
        reload();
        mlr = ldg(rws, vml, sml);
        if (__all((tagdiff(mlr, tag) & 0xffu) == 0u)) break;
        __builtin_amdgcn_s_sleep(OSLEEP);
      }
    }
    STAMP(1);
    if (OPRE) __builtin_amdgcn_s_sleep(OPRE);
    const int vo = (lane >> 5) * 2048 + (lane & 31) * 16, so = WS_OG + (kvh * 8 + c) * 8192 + j * 512;
    v4u a0 = ldg(rws, vo, so), a1 = ldg(rws, vo, so + 4096);
    pin();
    const float m = dec(mlr.x), M = max32(fmaxf(m, dpp<XOR1>(m)));
    const float e = ex2((m - M) * LOG2E);
    float Ls = e * dec(mlr.y);
    Ls = sum32(Ls + dpp<XOR1>(Ls));
    const float wt = e * __builtin_amdgcn_rcpf(Ls), w0 = dpp<0xA0>(wt), w1 = dpp<0xF5>(wt);
    if (WO_LATE && c >= 4) {
      await<1>(L, C_WO, 8);
#pragma unroll
      for (int s = 0; s < 4; ++s) wf[s] = wfrag(L + L_WO + lfr + s4 * 32, s);
    }
    pin();
    if (!__all(((tagdiff(a0, tag) | tagdiff(a1, tag)) & 0xffu) == 0u)) {
#pragma unroll 1
      for (unsigned spin = 0; spin < SPIN_CAP; ++spin) {
        __builtin_amdgcn_s_sleep(OSLEEP);
        reload();
        a0 = ldg(rws, vo, so); a1 = ldg(rws, vo, so + 4096);
        if (__all(((tagdiff(a0, tag) | tagdiff(a1, tag)) & 0xffu) == 0u)) break;
      }
    }
    const f32x4 o = f32x4{dec(a0.x), dec(a0.y), dec(a0.z), dec(a0.w)} * w0 + f32x4{dec(a1.x), dec(a1.y), dec(a1.z), dec(a1.w)} * w1;
    *(LAS unsigned*)(L + L_A + c * A_LD + (lane & 31) * 8 + (lane >> 5) * 4) = pk(tsum32(o[0], o[2]), tsum32(o[1], o[3]));
  }
  STAMP(11);
  wg_barrier();
  STAMP(12);
  if (c >= 4) {
#pragma unroll
    for (int s = 0; s < 4; ++s) yd = mfma(wf[s], *(LAS v4u*)(ap + s * 64), yd);
    if (!(lane & 8)) stg(rws, (lane & 7) * 64 + g * 16, WS_YG + (b * 32 + cc) * 2048 + s4 * 512, yd, tag);
  }
  STAMP(13);
  if (c == 0) {
    __builtin_amdgcn_s_sleep(YPRE);
    const f32x4 y = gather_sum<2, YSLEEP>(rws, (lane >> 2) * 2048 + (lane & 3) * 16, WS_YG + kvh * 65536 + j * 512 + b * 64, 32768, tag);
    float t = tsum16(tsum32(y[0], y[2]), tsum32(y[1], y[3]));
    t += dpp<ROR4>(t); t += dpp<ROR8>(t);
    if (!(lane & 12)) *(__attribute__((address_space(1))) __bf16*)(out + b * 1024 + cc * 32 + ((lane & 3) * 4 + g) * 2) = (__bf16)(lo16(xo) + t);
    if (bid == 0 && lane == 0) __builtin_amdgcn_raw_buffer_store_b32(epoch, rws, 0, WS_EPOCH, 16);
    STAMP(14);
  }
  STAMP_OUT();
}

extern "C" void kernel_launch(void* const* d_in, const int* in_sizes, int n_in, void* d_out, int out_size, void* d_ws, size_t ws_size, hipStream_t stream) {
  fused_decode<<<256, 1024, 0, stream>>>((const unsigned char*)d_in[1], (unsigned char*)d_ws, (const unsigned char*)d_in[0], (const unsigned char*)d_in[3],
                                          (const unsigned char*)d_in[6], (const unsigned char*)d_in[2], (const unsigned char*)d_in[7],
                                          (const unsigned char*)d_in[4], (unsigned char*)d_out);
}
```

```cpp
#include <hip/hip_runtime.h>
#include <hip/hip_bf16.h>
#include <stdint.h>

#define CHL 3
#define CHH 5
#define N0 8
#define LPOS 32
#define QTRIG 16
#define QTRIGH 11
#define QTRIGS 14
#define WTRIG 6
#define VPRE 6
#define WO_DLY 8
#define OPRE 4
#define OSLEEP 4
#define CODE_BYTES 13824

typedef unsigned v4u __attribute__((ext_vector_type(4)));
typedef unsigned v2u __attribute__((ext_vector_type(2)));
typedef float f32x4 __attribute__((ext_vector_type(4)));
typedef float f32x2 __attribute__((ext_vector_type(2)));
typedef short v4s __attribute__((ext_vector_type(4)));
typedef __bf16 bf16x8 __attribute__((ext_vector_type(8)));
typedef __bf16 bf16x2 __attribute__((ext_vector_type(2)));
#define LAS __attribute__((address_space(3)))
typedef __amdgpu_buffer_rsrc_t rsrc_t;

constexpr int SK = 8192, WIN = 2048;
constexpr int NT = 2, SC1 = 16;
constexpr int OOR = 0x7fffff00;
constexpr unsigned SPIN_CAP = 1u << 16;
constexpr float LOG2E = 1.4426950408889634f;
constexpr int WS_EPOCH = 0;
constexpr int WS_QG = 4096;
constexpr int WS_HG = WS_QG + 73 * 2048;
constexpr int WS_ML = WS_HG + 35 * 2048;
constexpr int WS_OG = WS_ML + 64 * 512;
constexpr int WS_YG = WS_OG + 256 * 1024;
constexpr int WS_AC = 3 << 19;
constexpr int WS_FL = 1024;
constexpr int WS_END = WS_AC + 8 * 4096;
static_assert(WS_YG + 256 * 2048 <= WS_AC, "ws");
constexpr int L_CNT = 0;
constexpr int L_ML = 64;
constexpr int L_Q = L_ML + 256;
constexpr int A_LD = 400;
constexpr int L_A = L_Q + 1024;
constexpr int L_OR = L_A + 8 * A_LD;
constexpr int W_LD = 144;
constexpr int L_WO = L_OR + 16384;
constexpr int XN_LD = 1040;
constexpr int L_XN = L_WO + 192 * W_LD;
constexpr int L_WQ = L_XN + 8 * XN_LD;
constexpr int L_END = L_WQ + 512 * W_LD;
constexpr int LDS_BYTES = 132 * 1024;
static_assert(L_END <= LDS_BYTES && L_WQ % 16 == 0 && L_WO % 16 == 0 && L_XN % 16 == 0 && L_A % 16 == 0 && L_Q % 16 == 0 && L_OR % 16 == 0, "lds");
enum { C_XN = 0, C_Q, C_ML, C_K, C_K2, C_W, C_V, C_WO, C_RD, C_WI };

__device__ __forceinline__ rsrc_t mkrs(const void* p, int bytes) { return __builtin_amdgcn_make_buffer_rsrc((void*)p, 0, bytes, 0x00020000); }
template <int N> __device__ __forceinline__ void waitvm() { asm volatile("s_waitcnt vmcnt(%0)" ::"n"(N) : "memory"); }
__device__ __forceinline__ void pin() { __builtin_amdgcn_sched_barrier(0); }
__device__ __forceinline__ void wg_barrier() { pin(); asm volatile("s_waitcnt lgkmcnt(0)\n\ts_barrier" ::: "memory"); pin(); }
__device__ __forceinline__ void reload() { asm volatile("" ::: "memory"); }
__device__ __forceinline__ float asf(unsigned u) { return __builtin_bit_cast(float, u); }
__device__ __forceinline__ unsigned asu(float f) { return __builtin_bit_cast(unsigned, f); }
__device__ __forceinline__ float lo16(unsigned u) { return asf(u << 16); }
__device__ __forceinline__ float hi16(unsigned u) { return asf(u & 0xffff0000u); }
__device__ __forceinline__ float rbf(float f) { return (float)(__bf16)f; }
__device__ __forceinline__ unsigned pk(float a, float b) { bf16x2 v = {(__bf16)a, (__bf16)b}; return __builtin_bit_cast(unsigned, v); }
template <int CTRL> __device__ __forceinline__ float dpp(float x) { return asf(__builtin_amdgcn_mov_dpp(asu(x), CTRL, 0xf, 0xf, true)); }
constexpr int XOR1 = 0xB1, XOR2 = 0x4E, HMIR = 0x141, ROR4 = 0x124, ROR8 = 0x128;
__device__ __forceinline__ float tsum16(float a, float b) { auto s = __builtin_amdgcn_permlane16_swap(asu(a), asu(b), false, false); return asf(s[0]) + asf(s[1]); }
__device__ __forceinline__ float tsum32(float a, float b) { auto s = __builtin_amdgcn_permlane32_swap(asu(a), asu(b), false, false); return asf(s[0]) + asf(s[1]); }
__device__ __forceinline__ float max16(float x) { auto s = __builtin_amdgcn_permlane16_swap(asu(x), asu(x), false, false); return fmaxf(asf(s[0]), asf(s[1])); }
__device__ __forceinline__ float max32(float x) { auto s = __builtin_amdgcn_permlane32_swap(asu(x), asu(x), false, false); return fmaxf(asf(s[0]), asf(s[1])); }
__device__ __forceinline__ float wavesum(float x) { x += dpp<XOR1>(x); x += dpp<XOR2>(x); x += dpp<ROR4>(x); x += dpp<ROR8>(x); x = tsum16(x, x); return tsum32(x, x); }
__device__ __forceinline__ float ex2(float x) { return __builtin_amdgcn_exp2f(x); }
__device__ __forceinline__ f32x4 mfma(v4u a, v4u b, f32x4 c) { return __builtin_amdgcn_mfma_f32_16x16x32_bf16(__builtin_bit_cast(bf16x8, a), __builtin_bit_cast(bf16x8, b), c, 0, 0, 0); }
__device__ __forceinline__ unsigned plo(unsigned a, unsigned b) { return __builtin_amdgcn_perm(b, a, 0x05040100u); }
__device__ __forceinline__ unsigned phi(unsigned a, unsigned b) { return __builtin_amdgcn_perm(b, a, 0x07060302u); }
__device__ __forceinline__ v4u ldw(rsrc_t r, int vo, int so) { return __builtin_amdgcn_raw_buffer_load_b128(r, vo, so, NT); }
__device__ __forceinline__ unsigned enc(float f, unsigned tag) { return __builtin_amdgcn_perm(asu(f) + 0x80u, tag, 0x07060500u); }
__device__ __forceinline__ v4u ldg(rsrc_t r, int vo, int so, unsigned tag) { return __builtin_amdgcn_raw_buffer_load_b128(r, vo, so, SC1) ^ tag; }
__device__ __forceinline__ unsigned orall(v4u v) { return (v.x | v.y) | (v.z | v.w); }
__device__ __forceinline__ bool fresh(unsigned m) { return __all((m & 0xffu) == 0u); }
__device__ __forceinline__ f32x4 asf4(v4u v) { return __builtin_bit_cast(f32x4, v); }
__device__ __forceinline__ void stg(rsrc_t r, int vo, int so, f32x4 d, unsigned tag) { __builtin_amdgcn_raw_buffer_store_b128(v4u{enc(d[0], tag), enc(d[1], tag), enc(d[2], tag), enc(d[3], tag)}, r, vo, so, SC1); }
__device__ __forceinline__ unsigned long long tofix(float y) {
  const float t = y * 1024.0f;
  unsigned hi, lo;
  asm("v_cvt_flr_i32_f32 %0, %1" : "=v"(hi) : "v"(t));
  asm("v_cvt_u32_f32 %0, %1" : "=v"(lo) : "v"(__builtin_amdgcn_fractf(t) * 4294967296.0f));
  lo = (lo & 0xfffff000u) | (fabsf(y) < 32768.0f ? 1u : 65u);
  return ((unsigned long long)hi << 32) | lo;
}
__device__ __forceinline__ float fromfix(unsigned long long t) { return fmaf((float)((unsigned)t & 0xfffff000u), 0x1p-42f, (float)(int)(t >> 32) * 0x1p-10f); }
__device__ __forceinline__ void signal(LAS unsigned char* L, int k, int lane) {
  asm volatile("s_waitcnt lgkmcnt(0)" ::: "memory");
  if (lane == 0) __hip_atomic_fetch_add((LAS unsigned*)(L + L_CNT + 4 * k), 1u, __ATOMIC_RELAXED, __HIP_MEMORY_SCOPE_WORKGROUP);
}
template <int SLP> __device__ __forceinline__ void await(LAS unsigned char* L, int k, unsigned n) {
#pragma unroll 1
  for (unsigned spin = 0; spin < (SPIN_CAP << 4); ++spin) {
    if ((unsigned)__builtin_amdgcn_readfirstlane(__hip_atomic_load((LAS unsigned*)(L + L_CNT + 4 * k), __ATOMIC_RELAXED, __HIP_MEMORY_SCOPE_WORKGROUP)) >= n) break;
    __builtin_amdgcn_s_sleep(SLP);
  }
  reload();
}
__device__ __forceinline__ v4u wfrag(LAS unsigned char* wp, int s) {
  const v2u lo = __builtin_bit_cast(v2u, __builtin_amdgcn_ds_read_tr16_b64_v4i16((LAS v4s*)(wp + s * 32 * W_LD)));
  const v2u hi = __builtin_bit_cast(v2u, __builtin_amdgcn_ds_read_tr16_b64_v4i16((LAS v4s*)(wp + (s * 32 + 4) * W_LD)));
  return v4u{lo.x, lo.y, hi.x, hi.y};
}

__global__ void __launch_bounds__(1024, 4)
fused_decode(const unsigned char* kc_, unsigned char* ws_, const unsigned char* x_, const unsigned char* wq_, const unsigned char* w1_,
             const unsigned char* vc_, const unsigned char* w2_, const unsigned char* wo_, unsigned char* out_) {
  typedef unsigned long long v8p __attribute__((ext_vector_type(8)));
  v8p ka8;
  asm volatile("s_load_dwordx16 %0, %1, 0x0" : "=s"(ka8) : "s"(__builtin_amdgcn_kernarg_segment_ptr()));
  pin();
  unsigned long long pc;
  asm volatile("s_getpc_b64 %0" : "=s"(pc));
  __shared__ __attribute__((aligned(16))) unsigned char smem[LDS_BYTES];
  LAS unsigned char* L = (LAS unsigned char*)smem;
  const int bid = blockIdx.x, kvh = bid & 7, r = bid >> 3, b = r & 7, j = r >> 3;
  const int tid = threadIdx.x, lane = tid & 63, w = __builtin_amdgcn_readfirstlane(tid >> 6);
  const int g = lane >> 4, vv = lane * 16;
  const int hi = bid - 72;
  const bool do_q = bid < 72 || bid == 107, heavy = bid < 108;
#define SPLIT (do_q ? (r == 3 || r >= 8 || bid == 63) : ((hi & 3) == 3 || hi >= 28))
  const int c = w & 7;
  const int rot = (int)(((unsigned)bid * 2654435761u) >> 20) + c;
  v4u K[16], V[16];
#define PIECE(i) ((((i) * 8 + rot) & 127) << 10)
#define LDK(i) K[i] = __builtin_amdgcn_raw_buffer_load_b128(rk, vk, PIECE(i), NT)
#define LDV(i) V[i] = __builtin_amdgcn_raw_buffer_load_b128(rv, vv, PIECE(i), NT)
#define ARG(i) ((unsigned char*)ka8[i])
  unsigned epv;
  int ld = 0, nwb = 0, va, sa, sb = 0;
  v4u xr, wt[8];
  if (w < 8) {
    if (bid >= 248 && w < 2) {
      const unsigned t = __builtin_amdgcn_raw_buffer_load_b32(mkrs((const void*)pc, CODE_BYTES), tid * 128, 0, 0);
      asm volatile("" ::"v"(t));
    }
    sa = c * 1024; va = 0;
    if (heavy) {
      int h0 = hi, e107 = (bid ^ 107) - 1;
      asm volatile("" : "+s"(h0), "+s"(e107));
      const int qm = (h0 | e107) >> 31;
      const int colq = bid == 107 ? 63 : kvh * 8 + (r < 8 ? r : 3), colh = hi < 28 ? hi : 4 * hi - 109;
      const int offq = bid == 63 ? 4 : (int)((0x22224222ull >> (4 * r)) & 15), offh = hi >= 28 ? 0 : (hi & 3) == 3 ? 4 : 2;
      ld = 3584 + (qm & 4608);
      nwb = ((offq & qm) | (offh & ~qm)) == (c & 4) ? 0 : 512 * ld;
      va = (lane >> 3) * ld + (lane & 7) * 16; sb = c * 64 * ld + ((colq & qm) | (colh & ~qm)) * 128;
    }
  } else {
    sa = ((b * 8 + kvh) * SK + (SK - WIN) + j * 512) * 256;
    va = ((lane >> 2) & 3) * 256 + g * 64 + (lane & 3) * 16;
    sb = PIECE(0); ld = PIECE(1);
  }
  int w1_0 = w, vx = vv;
  asm volatile("" : "+s"(ld), "+s"(nwb), "+s"(sa), "+s"(sb), "+s"(w1_0));
  asm volatile("" : "+v"(va), "+v"(vx));
  pin();
  asm volatile("s_waitcnt lgkmcnt(0)" : "+s"(ka8));
  if (w1_0 < 8) {
    if (heavy) {
      xr = __builtin_amdgcn_raw_buffer_load_b128(mkrs(ARG(2), 8192), vx, sa, 0);
      const rsrc_t r0 = mkrs(do_q ? ARG(3) : ARG(4), nwb);
#pragma unroll
      for (int i = 0; i < 3; ++i) wt[i] = ldw(r0, va, sb + i * 8 * ld);
    }
    epv = __builtin_amdgcn_raw_buffer_load_b32(mkrs(ARG(1), 4), 0, WS_EPOCH, SC1);
  } else if (!heavy) {
    const rsrc_t r0 = mkrs(ARG(0) + (unsigned)sa, 131072);
    K[0] = __builtin_amdgcn_raw_buffer_load_b128(r0, va, sb, NT);
    K[1] = __builtin_amdgcn_raw_buffer_load_b128(r0, va, ld, NT);
  }
  pin();
  {
    int z = 0, t4 = tid < 16 ? L_CNT + 4 * tid : L_Q;
    asm volatile("" : "+v"(z), "+v"(t4));
    if (w == 0) *(LAS int*)(L + t4) = z;
  }
  const int vk = va, vb = va;
  const unsigned kvoff = sa;
  wg_barrier();
  int wr = w;
  asm volatile("" : "+s"(wr));
  const unsigned char *x = ARG(2), *vc = ARG(5), *w2 = ARG(6), *wo = ARG(7);
  unsigned char* ws = ARG(1);
  const rsrc_t rk = mkrs(ARG(0) + kvoff, 131072), rws = mkrs(ws, WS_END), rx = mkrs(x, 8 * 512 * 2), rwb = mkrs(do_q ? ARG(3) : ARG(4), nwb);
  const bool split = SPLIT;

  if (wr >= 8) {
    const rsrc_t rv = mkrs(vc + kvoff, 131072);
#define WAITN(n) do { if ((n) >= LPOS || heavy) waitvm<CHH - 1>(); else if ((n) < N0) waitvm<1>(); else waitvm<CHL - 1>(); } while (0)
#define TRIG(n) do { if (((n) == QTRIG && !heavy) || ((n) == QTRIGH && heavy && !split) || ((n) == QTRIGS && heavy && split)) signal(L, C_K, lane); if ((n) == WTRIG) signal(L, C_K2, lane); } while (0)
    if (heavy) {
      await<2>(L, C_WI, 8);
      LDK(0); LDK(1);
    }
#pragma unroll
    for (int i = 2; i < 16; ++i) { WAITN(i); LDK(i); TRIG(i); pin(); }
#pragma unroll
    for (int i = 0; i < VPRE; ++i) { WAITN(16 + i); LDV(i); TRIG(16 + i); pin(); }
    await<1>(L, C_Q, 1);
    const v4u qb = *(LAS v4u*)(L + L_Q + vk);
    float s[16];
#pragma unroll
    for (int i4 = 0; i4 < 4; ++i4) {
#pragma unroll
      for (int i = 4 * i4; i < 4 * i4 + 4; i += 2) {
        const f32x4 d = mfma(K[i], qb, f32x4{0.f, 0.f, 0.f, 0.f}), e = mfma(K[i + 1], qb, f32x4{0.f, 0.f, 0.f, 0.f});
        const float v0 = (dpp<0x00>(d[0]) + dpp<0x55>(d[1])) + (dpp<0xAA>(d[2]) + dpp<0xFF>(d[3]));
        const float v1 = (dpp<0x00>(e[0]) + dpp<0x55>(e[1])) + (dpp<0xAA>(e[2]) + dpp<0xFF>(e[3]));
        const unsigned u = pk(v0, v1);
        const unsigned t = pk(lo16(u) * 0.088388347648318440f, hi16(u) * 0.088388347648318440f);
        s[i] = lo16(t); s[i + 1] = hi16(t);
      }
      pin();
      LDV(VPRE + i4);
      pin();
    }
    float mw = fmaxf(s[0], s[1]);
#pragma unroll
    for (int i = 2; i < 16; i += 2) mw = fmaxf(fmaxf(mw, s[i]), s[i + 1]);
    mw = max32(max16(mw));
    float lw = 0.f;
#pragma unroll
    for (int i = 0; i < 16; ++i) { s[i] = ex2((s[i] - mw) * LOG2E); lw += s[i]; }
    lw = tsum16(lw, lw);
    lw = tsum32(lw, lw);
    const v4u pa0 = {pk(s[0], s[1]), pk(s[2], s[3]), pk(s[4], s[5]), pk(s[6], s[7])};
    const v4u pa1 = {pk(s[8], s[9]), pk(s[10], s[11]), pk(s[12], s[13]), pk(s[14], s[15])};
    if ((lane & 0x33) == 0) *(LAS f32x2*)(L + L_ML + lane * 16 + c * 8) = f32x2{mw, lw};
    signal(L, C_ML, lane);
    pin();
#pragma unroll
    for (int i = VPRE + 4; i < 13; ++i) { WAITN(16 + i); LDV(i); pin(); }
    f32x4 acc[8];
#pragma unroll
    for (int d = 0; d < 4; ++d) {
      acc[2 * d] = mfma(pa0, v4u{plo(V[0][d], V[1][d]), plo(V[2][d], V[3][d]), plo(V[4][d], V[5][d]), plo(V[6][d], V[7][d])}, f32x4{0.f, 0.f, 0.f, 0.f});
      acc[2 * d + 1] = mfma(pa0, v4u{phi(V[0][d], V[1][d]), phi(V[2][d], V[3][d]), phi(V[4][d], V[5][d]), phi(V[6][d], V[7][d])}, f32x4{0.f, 0.f, 0.f, 0.f});
    }
    pin();
#pragma unroll
    for (int i = 13; i < 16; ++i) { WAITN(16 + i); LDV(i); pin(); }
    signal(L, C_V, lane);
    unsigned bt[8][4];
#pragma unroll
    for (int p = 0; p < 4; ++p) {
#pragma unroll
      for (int d = 0; d < 4; ++d) { bt[2 * d][p] = plo(V[8 + 2 * p][d], V[9 + 2 * p][d]); bt[2 * d + 1][p] = phi(V[8 + 2 * p][d], V[9 + 2 * p][d]); }
      pin();
    }
#pragma unroll
    for (int e = 0; e < 8; ++e) acc[e] = mfma(pa1, v4u{bt[e][0], bt[e][1], bt[e][2], bt[e][3]}, acc[e]);
    {
      LAS f32x4* p = (LAS f32x4*)(L + L_OR + c * 2048 + lane * 32);
      p[0] = f32x4{acc[0][0], acc[1][0], acc[2][0], acc[3][0]};
      p[1] = f32x4{acc[4][0], acc[5][0], acc[6][0], acc[7][0]};
    }
    wg_barrier();
    return;
  }

  const int s4 = c & 3;
  const int cc = kvh * 4 + j;
  const int lrow = (lane >> 3) * W_LD + (lane & 7) * 16;
  const int lfr = (g * 8 + ((lane >> 2) & 3)) * W_LD + (lane & 3) * 8;
  if (heavy) {
#pragma unroll
    for (int i = 3; i < 8; ++i) wt[i] = ldw(rwb, vb, sb + i * 8 * ld);
    pin();
    signal(L, C_WI, lane);
    {
      float xv[8];
#pragma unroll
      for (int k = 0; k < 4; ++k) { xv[2 * k] = lo16(xr[k]); xv[2 * k + 1] = hi16(xr[k]); }
      const float mean = wavesum(((xv[0] + xv[1]) + (xv[2] + xv[3])) + ((xv[4] + xv[5]) + (xv[6] + xv[7]))) * (1.0f / 512.0f);
      float q = 0.f;
#pragma unroll
      for (int k = 0; k < 8; ++k) { const float d = xv[k] - mean; q = fmaf(d, d, q); }
      const float var = rbf(wavesum(q) * (1.0f / 512.0f)), mb = rbf(mean);
      const float rden = __builtin_amdgcn_rcpf(rbf(__builtin_amdgcn_sqrtf(rbf(var + 1e-5f))));
      v4u xa;
#pragma unroll
      for (int k = 0; k < 4; ++k) xa[k] = pk(rbf(xv[2 * k] - mb) * rden, rbf(xv[2 * k + 1] - mb) * rden);
      *(LAS v4u*)(L + L_XN + c * XN_LD + vv) = xa;
    }
    signal(L, C_XN, lane);
    LAS unsigned char* const wrow = L + L_WQ + c * 64 * W_LD;
    await<1>(L, C_XN, 8);
    {
      LAS unsigned char* ap = L + L_XN + (lane & 7) * XN_LD + c * 128 + g * 16;
      const v4u x0 = *(LAS v4u*)ap, x1 = *(LAS v4u*)(ap + 64);
      f32x4 d[4];
#pragma unroll
      for (int i = 0; i < 4; ++i) *(LAS v4u*)(wrow + i * 8 * W_LD + lrow) = wt[i];
#pragma unroll
      for (int t = 0; t < 4; ++t) d[t] = mfma(wfrag(wrow + lfr + t * 32, 0), x0, f32x4{0.f, 0.f, 0.f, 0.f});
      pin();
#pragma unroll
      for (int i = 4; i < 8; ++i) *(LAS v4u*)(wrow + i * 8 * W_LD + lrow) = wt[i];
#pragma unroll
      for (int t = 0; t < 4; ++t) d[t] = mfma(wfrag(wrow + lfr + t * 32, 1), x1, d[t]);
      asm volatile("" : "+v"(d[0]), "+v"(d[1]), "+v"(d[2]), "+v"(d[3])::"memory");
      LAS unsigned char* rp = wrow + (lane & 7) * 272 + (lane & 8) * 8 + g * 16;
      *(LAS f32x4*)rp = (lane & 8) ? d[1] : d[0];
      *(LAS f32x4*)(rp + 128) = (lane & 8) ? d[3] : d[2];
    }
    signal(L, C_RD, lane);
    if (c >= 6) {
      await<1>(L, C_RD, 8);
      const int ro = L_WQ + ((c & 1) * 4 + g) * 272 + (lane & 15) * 16;
      f32x4 d = *(LAS f32x4*)(L + ro);
#pragma unroll
      for (int k = 1; k < 8; ++k) d += *(LAS f32x4*)(L + ro + k * 64 * W_LD);
      if (!split) d = f32x4{rbf(d[0]), rbf(d[1]), rbf(d[2]), rbf(d[3])};
      stg(rws, vv, (do_q ? WS_QG + (bid == 107 ? 72 : kvh * 9 + r) * 2048 : WS_HG + hi * 2048) + (c & 1) * 1024, d, (__builtin_amdgcn_readfirstlane(epv) + 1u) & 0xffu);
    }
  }
  const unsigned epo = __builtin_amdgcn_readfirstlane(epv), epoch = ((epo + 1u) & ~0x40004000u) | (~epo & 0x40004000u), tag = epoch & 0xffu, tag2 = epoch & 0x40004000u;
  if (r == 0 && c == 6) {
#pragma unroll
    for (int k = 0; k < 4; ++k) __builtin_amdgcn_raw_buffer_store_b128(v4u{0u, 0u, 0u, 0u}, rws, vv, WS_AC + kvh * 4096 + k * 1024, SC1);
    waitvm<0>();
    if (lane == 0) __builtin_amdgcn_raw_buffer_store_b128(v4u{epoch, epoch, epoch, epoch}, rws, 0, WS_FL + kvh * 128, SC1);
  }
  typedef unsigned char* ptr_t;
  const __attribute__((address_space(4))) ptr_t* ka = (const __attribute__((address_space(4))) ptr_t*)__builtin_amdgcn_kernarg_segment_ptr();
  asm volatile("" : "+s"(ka));
  unsigned char* out = ka[8];
  asm volatile("" ::"s"(out));
  await<4>(L, C_K2, 1);
  const int vwo = (lane >> 3) * 1024 + (lane & 7) * 16;
  const int rec = (kvh * 8 + b) * 4 + j;
  const int b0 = 4 * (g & 1) + (g >> 1);
  unsigned xo0 = 0, xo1 = 0;
  {
    const v4u t = ldw(mkrs(w2, c < 7 ? 1792 * 1024 : 0), vwo, (cc * 56 + c * 8) * 1024 + b * 128);
    if (c >= 4) {
      xo0 = __builtin_amdgcn_raw_buffer_load_b16(rx, b0 * 1024 + (lane & 15) * 2, b * 128 + s4 * 32, 0);
      xo1 = __builtin_amdgcn_raw_buffer_load_b16(rx, b0 * 1024 + (lane & 15) * 2, b * 128 + s4 * 32 + 2048, 0);
    }
    *(LAS v4u*)(L + L_WO + (128 + c * 8) * W_LD + lrow) = t;
  }
  if (c == 0) {
    await<4>(L, C_K, 1);
    v4u a0, a1, a2, a3;
    const int vo = g * 2048 + (lane & 15) * 16, so = WS_QG + kvh * 9 * 2048 + b * 256;
#pragma unroll 1
    for (unsigned spin = 0; spin < SPIN_CAP; ++spin) {
      reload();
      a0 = ldg(rws, vo, so, tag); a1 = ldg(rws, vo, so + 8192, tag);
      a2 = ldg(rws, (lane & 15) * 16, so + 16384, tag); a3 = ldg(rws, (lane & 15) * 16, so + (kvh == 7 ? 18432 : 16384), tag);
      if (fresh(orall(a0) | orall(a1) | orall(a2) | orall(a3))) break;
      __builtin_amdgcn_s_sleep(16);
    }
    const f32x4 q0 = asf4(a0) + asf4(g == 3 ? a2 : v4u{0u, 0u, 0u, 0u}), q1 = asf4(a1) + asf4(g == 3 && kvh == 7 ? a3 : v4u{0u, 0u, 0u, 0u});
    *(LAS v2u*)(L + L_Q + lane * 8) = v2u{pk(q0[0], q0[1]), pk(q0[2], q0[3])};
    *(LAS v2u*)(L + L_Q + 512 + lane * 8) = v2u{pk(q1[0], q1[1]), pk(q1[2], q1[3])};
    signal(L, C_Q, lane);
  } else if (c == 2 || c == 3) {
    await<8>(L, C_Q, 1);
    const int u = 56 * cc + 4 * min(lane & 15, 13), ub = u >> 6;
    const bool two = (ub & 3) == 3;
    const int vo = g * 256 + (u & 63) * 4, v0 = vo + ub * 2048, v1 = two ? vo + (28 + (ub >> 2)) * 2048 : v0, so = WS_HG + (c - 2) * 1024;
    v4u a0, a1;
#pragma unroll 1
    for (unsigned spin = 0; spin < SPIN_CAP; ++spin) {
      reload();
      a0 = ldg(rws, v0, so, tag); a1 = ldg(rws, v1, so, tag);
      if (fresh(orall(a0) | orall(a1))) break;
      __builtin_amdgcn_s_sleep(16);
    }
    const f32x4 h = asf4(a0) + asf4(two ? a1 : v4u{0u, 0u, 0u, 0u});
    *(LAS v2u*)(L + L_A + (4 * (c - 2) + g) * A_LD + 256 + (lane & 15) * 8) =
        (lane & 15) >= 14 ? v2u{0u, 0u} : v2u{pk(fmaxf(h[0], 0.f), fmaxf(h[1], 0.f)), pk(fmaxf(h[2], 0.f), fmaxf(h[3], 0.f))};
  }
  signal(L, C_W, lane);
  if (c == 0) {
    await<8>(L, C_ML, 4);
#pragma unroll 1
    for (unsigned spin = 0; spin < SPIN_CAP; ++spin) {
      reload();
      if (__all(orall(ldg(rws, 0, WS_FL + b * 128, epoch)) == 0u)) break;
      __builtin_amdgcn_s_sleep(16);
    }
  }
  if (c == 5) {
    unsigned t0 = __builtin_amdgcn_raw_buffer_load_b32(rws, 0, WS_ML + rec * 128, SC1);
    asm volatile("" ::"v"(t0));
    await<8>(L, C_Q, 1);
    t0 = __builtin_amdgcn_raw_buffer_load_b32(rws, (lane & 7) * 128, WS_OG + rec * 1024, SC1);
    asm volatile("" ::"v"(t0));
  }
  f32x4 yd = {0.f, 0.f, 0.f, 0.f};
  int la = L_A + (lane & 7) * A_LD + g * 16, lw = L_WO + lfr + s4 * 32;
  if (c >= 4) {
    await<4>(L, C_W, 8);
#pragma unroll
    for (int s = 4; s < 6; ++s) yd = mfma(*(LAS v4u*)(L + la + s * 64), wfrag(L + lw, s), yd);
  }
  await<4>(L, C_ML, 8);
  v4u mlo = {0u, 0u, 0u, 0u};
  if (c == 1) {
    const f32x2 ml = *(LAS f32x2*)(L + L_ML + (lane & 31) * 8);
    float M = ml[0];
    M = fmaxf(M, dpp<XOR1>(M)); M = fmaxf(M, dpp<XOR2>(M)); M = fmaxf(M, dpp<HMIR>(M));
    float Ls = ex2((ml[0] - M) * LOG2E) * ml[1];
    Ls += dpp<XOR1>(Ls); Ls += dpp<XOR2>(Ls); Ls += dpp<HMIR>(Ls);
    mlo = v4u{enc(M, tag), enc(Ls, tag), epoch, epoch};
  }
  int vs = (c == 1 && (lane & 0x27) == 0) ? lane * 2 : OOR, ss = WS_ML + rec * 128;
  asm volatile("" : "+v"(vs), "+v"(mlo));
  asm volatile("" : "+s"(ss));
  float sc[4];
  {
    f32x4 t[4];
#pragma unroll
    for (int k = 0; k < 4; ++k) t[k] = *(LAS f32x4*)(L + L_ML + (c >> 1) * 64 + k * 16);
    const float M = fmaxf(fmaxf(fmaxf(t[0][0], t[0][2]), fmaxf(t[1][0], t[1][2])), fmaxf(fmaxf(t[2][0], t[2][2]), fmaxf(t[3][0], t[3][2])));
#pragma unroll
    for (int k = 0; k < 4; ++k) sc[k] = ex2(((lane < 32 ? t[k >> 1][2 * (k & 1)] : t[2 + (k >> 1)][2 * (k & 1)]) - M) * LOG2E) * 0x1p-64f;
  }
  int lm = L_OR + (lane >> 5) * 8192 + c * 256 + (lane & 31) * 8, vm = lane < 32 ? lane * 4 : OOR, sm = WS_OG + rec * 1024 + c * 128;
  int vo = g * 1024 + (lane & 15) * 16, so = WS_OG + (kvh * 8 + c) * 4096 + j * 256, lo = L_A + c * A_LD + (lane & 15) * 16 + g * 4;
  int vy = lane * 8, sy = WS_YG + (b * 32 + cc) * 2048 + s4 * 512;
  int vz = b0 * 1024 + (lane & 15) * 2, sz = b * 128 + s4 * 32;
  float xf0 = lo16(xo0), xf1 = lo16(xo1);
  typedef __attribute__((address_space(1))) unsigned long long gu64;
  gu64* ac = (gu64*)(ws + WS_AC + b * 4096 + b0 * 512 + s4 * 128 + (lane & 15) * 8);
  const rsrc_t rout = mkrs(out, 8192);
  asm volatile("" : "+v"(sc[0]), "+v"(sc[1]), "+v"(sc[2]), "+v"(sc[3]), "+v"(lm), "+v"(vm), "+v"(vo),
               "+v"(lo), "+v"(vy), "+v"(vz), "+v"(xf0), "+v"(xf1), "+v"(la), "+v"(lw), "+v"(ac));
  asm volatile("" : "+s"(sm), "+s"(so), "+s"(sy), "+s"(sz));
  await<2>(L, C_V, 8);
  __builtin_amdgcn_raw_buffer_store_b128(mlo, rws, vs, ss, SC1);
  if (WO_DLY) __builtin_amdgcn_s_sleep(WO_DLY);
  const rsrc_t rwo = mkrs(wo, 4096 * 1024);
  const v4u wa0 = ldw(rwo, vwo, (cc * 128 + c * 16) * 1024 + b * 128), wa1 = ldw(rwo, vwo, (cc * 128 + c * 16 + 8) * 1024 + b * 128);
  const int vml = g * 128, sml = WS_ML + (kvh * 8 + c) * 512 + j * 16;
  wg_barrier();
  {
    f32x2 p[4];
#pragma unroll
    for (int k = 0; k < 4; ++k) p[k] = *(LAS f32x2*)(L + lm + k * 2048);
    const f32x2 o = (p[0] * sc[0] + p[1] * sc[1]) + (p[2] * sc[2] + p[3] * sc[3]);
    const float o0 = __builtin_amdgcn_fmed3f(tsum32(o[0], o[0]), -1.984375f, 1.984375f), o1 = __builtin_amdgcn_fmed3f(tsum32(o[1], o[1]), -1.984375f, 1.984375f);
    __builtin_amdgcn_raw_buffer_store_b32(pk(o0, o1) | tag2, rws, vm, sm, SC1);
  }
  pin();
  *(LAS v4u*)(L + L_WO + (c * 16) * W_LD + lrow) = wa0;
  *(LAS v4u*)(L + L_WO + (c * 16 + 8) * W_LD + lrow) = wa1;
  signal(L, C_WO, lane);
#define ML_OK() __all(((((mlr.x ^ tag) | (mlr.y ^ tag)) & 0xffu) | (mlr.z ^ epoch) | (mlr.w ^ epoch)) == 0u)
  v4u wf[4];
  {
    if (OPRE) __builtin_amdgcn_s_sleep(OPRE);
    v4u mlr = __builtin_amdgcn_raw_buffer_load_b128(rws, vml, sml, SC1);
    v4u a0 = __builtin_amdgcn_raw_buffer_load_b128(rws, vo, so, SC1);
    pin();
    await<1>(L, C_WO, 8);
#pragma unroll
    for (int s = 0; s < 4; ++s) wf[s] = wfrag(L + lw, s);
    pin();
    if (!ML_OK()) {
#pragma unroll 1
      for (unsigned spin = 0; spin < SPIN_CAP; ++spin) {
        __builtin_amdgcn_s_sleep(OSLEEP);
        reload();
        mlr = __builtin_amdgcn_raw_buffer_load_b128(rws, vml, sml, SC1);
        if (ML_OK()) break;
      }
    }
    const float m = asf(mlr.x ^ tag), M = max32(max16(m));
    const float e = ex2((m - M) * LOG2E);
    float Ls = e * asf(mlr.y ^ tag);
    Ls = tsum16(Ls, Ls);
    Ls = tsum32(Ls, Ls);
    const float wt_ = e * __builtin_amdgcn_rcpf(Ls) * 0x1p64f;
    a0 ^= tag2;
    if (!__all((orall(a0) & 0x40004000u) == 0u)) {
#pragma unroll 1
      for (unsigned spin = 0; spin < SPIN_CAP; ++spin) {
        __builtin_amdgcn_s_sleep(OSLEEP);
        reload();
        a0 = ldg(rws, vo, so, tag2);
        if (__all((orall(a0) & 0x40004000u) == 0u)) break;
      }
    }
    const f32x4 el = f32x4{lo16(a0.x), lo16(a0.y), lo16(a0.z), lo16(a0.w)} * wt_, eh = f32x4{hi16(a0.x), hi16(a0.y), hi16(a0.z), hi16(a0.w)} * wt_;
    *(LAS unsigned*)(L + lo) = pk(tsum16(tsum32(el[0], el[2]), tsum32(el[1], el[3])), tsum16(tsum32(eh[0], eh[2]), tsum32(eh[1], eh[3])));
  }
  wg_barrier();
  if (c >= 4) {
    v4u oa[4];
#pragma unroll
    for (int s = 0; s < 4; ++s) oa[s] = *(LAS v4u*)(L + la + s * 64);
    pin();
#pragma unroll
    for (int s = 0; s < 4; ++s) yd = mfma(oa[s], wf[s], yd);
    const float y0 = lane < 32 ? yd[0] : yd[1], y1 = lane < 32 ? yd[2] : yd[3];
    const unsigned long long f0 = tofix(y0), f1 = tofix(y1);
    const unsigned long long t0 = __hip_atomic_fetch_add(ac, f0, __ATOMIC_RELAXED, __HIP_MEMORY_SCOPE_AGENT) + f0;
    const unsigned long long t1 = __hip_atomic_fetch_add(ac + 128, f1, __ATOMIC_RELAXED, __HIP_MEMORY_SCOPE_AGENT) + f1;
    __builtin_amdgcn_raw_buffer_store_b64(v2u{enc(y0, tag), enc(y1, tag)}, rws, vy, sy, SC1);
    const float r0 = xf0 + fromfix(t0), r1 = xf1 + fromfix(t1);
    const bool l0 = ((unsigned)t0 & 63u) == 32u, l1 = ((unsigned)t1 & 63u) == 32u;
    const bool k0 = ((unsigned)t0 & 0xfffu) == 32u && fmaxf(fabsf(r0), fabsf(xf0)) >= 0x1p-13f, k1 = ((unsigned)t1 & 0xfffu) == 32u && fmaxf(fabsf(r1), fabsf(xf1)) >= 0x1p-13f;
    const unsigned rr = pk(r0, r1);
    __builtin_amdgcn_raw_buffer_store_b16((unsigned short)rr, rout, k0 ? vz : OOR, sz, 0);
    __builtin_amdgcn_raw_buffer_store_b16((unsigned short)(rr >> 16), rout, k1 ? vz + 2048 : OOR, sz, 0);
    pin();
    const bool s0 = l0 && !k0, s1 = l1 && !k1;
    if (__any(s0 || s1)) {
      float a0 = 0.f, a1 = 0.f;
#pragma unroll 1
      for (int k = 0; k < 32; k += 8) {
        v2u p[8];
#pragma unroll 1
        for (unsigned spin = 0; spin < SPIN_CAP; ++spin) {
          reload();
          unsigned m = 0u;
#pragma unroll
          for (int i = 0; i < 8; ++i) { p[i] = __builtin_amdgcn_raw_buffer_load_b64(rws, vy, WS_YG + (b * 32 + k + i) * 2048 + s4 * 512, SC1) ^ tag; m |= p[i].x | p[i].y; }
          if (fresh(m)) break;
          __builtin_amdgcn_s_sleep(2);
        }
#pragma unroll
        for (int i = 0; i < 8; ++i) { a0 += asf(p[i].x); a1 += asf(p[i].y); }
      }
      const unsigned ra = pk(xf0 + a0, xf1 + a1);
      __builtin_amdgcn_raw_buffer_store_b16((unsigned short)ra, rout, s0 ? vz : OOR, sz, 0);
      __builtin_amdgcn_raw_buffer_store_b16((unsigned short)(ra >> 16), rout, s1 ? vz + 2048 : OOR, sz, 0);
    }
    if (b == 0 && c == 4 && lane == 0 && l0) __builtin_amdgcn_raw_buffer_store_b32(epoch, rws, 0, WS_EPOCH, SC1);
  }
}

extern "C" void kernel_launch(void* const* d_in, const int* in_sizes, int n_in, void* d_out, int out_size, void* d_ws, size_t ws_size, hipStream_t stream) {
  fused_decode<<<256, 1024, 0, stream>>>((const unsigned char*)d_in[1], (unsigned char*)d_ws, (const unsigned char*)d_in[0], (const unsigned char*)d_in[3],
                                          (const unsigned char*)d_in[6], (const unsigned char*)d_in[2], (const unsigned char*)d_in[7],
                                          (const unsigned char*)d_in[4], (unsigned char*)d_out);
}
```
